```python
import torch

def _softmax(x, axis=-1):
    return torch.softmax(x, dim=axis)
def _gelu(x):
    return torch.nn.functional.gelu(x, approximate="tanh")
def _silu(x):
    return torch.nn.functional.silu(x)
def _sigmoid(x):
    return torch.sigmoid(x)
def _layernorm(x, axis=-1, eps=1e-5):
    m = x.mean(dim=axis, keepdim=True)
    v = x.var(dim=axis, keepdim=True, unbiased=False)
    return (x - m) / torch.sqrt(v + eps)
def _scatter_add(tgt, idx, src, axis=0):
    return tgt.clone().index_add_(axis, idx, src)
def _segment_sum(src, seg, n):
    out = torch.zeros((n,) + src.shape[1:], dtype=src.dtype)
    return out.index_add_(0, seg, src)
def _pad(x, pw):
    flat = tuple(v for pair in reversed(pw) for v in pair)
    return torch.nn.functional.pad(x, flat)

kernel_name = 'sequential_preLN__mha_block_cached__glu_gated_geglu__decode'

def setup_inputs():

    _w = (lambda a: (lambda t: t.to(torch.bfloat16) if t.is_floating_point() else t)(torch.as_tensor(a)))
    def _A_setup():
        B, Sq, D = 1, 1, 576
        H, Sk, Dh = 32, 16384, 128
        return {
            "x": _w((lambda *s: torch.randn(*s, dtype=torch.bfloat16) * 0.05)(B, Sq, D)),
            "k_cache": _w((lambda *s: torch.randn(*s, dtype=torch.bfloat16) * 3.2)(B, H, Sk, Dh)),
            "v_cache": _w((lambda *s: torch.randn(*s, dtype=torch.bfloat16) * 1.6)(B, H, Sk, Dh)),
            "wq": _w((lambda *s: torch.randn(*s, dtype=torch.bfloat16) * 3.2)(D, H * Dh) * (D ** -0.5)),
            "wo": _w((lambda *s: torch.randn(*s, dtype=torch.bfloat16) * 1.6)(H * Dh, D) * ((H * Dh) ** -0.5)),
        }
    _A_in = _A_setup()

    def _B_setup():
        B, S, Dm, Df = 1, 1, 576, 4608
        return {
            "x": _w((lambda *s: torch.randn(*s, dtype=torch.bfloat16) * 0.05)(B, S, Dm)),
            "w_gate": _w((lambda *s: torch.randn(*s, dtype=torch.bfloat16) * 1.6)(Dm, Df) * (Dm ** -0.5)),
            "w_up": _w((lambda *s: torch.randn(*s, dtype=torch.bfloat16) * 1.6)(Dm, Df) * (Dm ** -0.5)),
            "w_down": _w((lambda *s: torch.randn(*s, dtype=torch.bfloat16) * 1.6)(Df, Dm) * (Df ** -0.5)),
        }
    _B_in = _B_setup()

    return {
        "A_x": _A_in["x"],
        "A_k_cache": _A_in["k_cache"],
        "A_v_cache": _A_in["v_cache"],
        "A_wq": _A_in["wq"],
        "A_wo": _A_in["wo"],
        "B_x": _B_in["x"],
        "B_w_gate": _B_in["w_gate"],
        "B_w_up": _B_in["w_up"],
        "B_w_down": _B_in["w_down"],
    }

def reference(A_x, A_k_cache, A_v_cache, A_wq, A_wo, B_x, B_w_gate, B_w_up, B_w_down):
    def _A_ref(x, k_cache, v_cache, wq, wo):
        B, Sq, D = x.shape
        H, Dh = 32, 128
        q = (lambda x, *d: x.permute(*d))((x @ wq).reshape(B, Sq, H, Dh), 0, 2, 1, 3)
        scores = torch.einsum("bhqd,bhkd->bhqk", q, k_cache) * (Dh ** -0.5)
        p = _softmax(scores, axis=-1)
        o = torch.einsum("bhqk,bhkd->bhqd", p, v_cache)
        return (lambda x, *d: x.permute(*d))(o, 0, 2, 1, 3).reshape(B, Sq, H * Dh) @ wo

    def _B_ref(x, w_gate, w_up, w_down):
        g = torch.einsum("bsd,df->bsf", x, w_gate)
        u = torch.einsum("bsd,df->bsf", x, w_up)
        h = _gelu(g) * u
        return torch.einsum("bsf,fd->bsd", h, w_down)

    x = A_x
    h = x + _A_ref(_layernorm(x, axis=-1), A_k_cache, A_v_cache, A_wq, A_wo)
    h2 = h + _B_ref(_layernorm(h, axis=-1), B_w_gate, B_w_up, B_w_down)
    return h2
```

```cpp
#include <hip/hip_runtime.h>
#include <hip/hip_bf16.h>
#include <stdint.h>

#define MIMIC 1

typedef unsigned short u16;
typedef unsigned u32x4 __attribute__((ext_vector_type(4)));
constexpr int D = 576, H = 32, SK = 16384, DH = 128, DF = 4608;
constexpr int NSPLIT = 16;

__device__ __forceinline__ float bf2f(u16 v) { return __uint_as_float((unsigned)v << 16); }
__device__ __forceinline__ float lo(unsigned v) { return __uint_as_float(v << 16); }
__device__ __forceinline__ float hi(unsigned v) { return __uint_as_float(v & 0xffff0000u); }
__device__ __forceinline__ float rbf(float f) {
  unsigned u = __float_as_uint(f);
  u += 0x7fffu + ((u >> 16) & 1u);
  return __uint_as_float(u & 0xffff0000u);
}
__device__ __forceinline__ float mr(float f) { return MIMIC ? rbf(f) : f; }

__device__ float block_sum(float v, float* red) {
  for (int o = 32; o > 0; o >>= 1) v += __shfl_xor(v, o);
  __syncthreads();
  if ((threadIdx.x & 63) == 0) red[threadIdx.x >> 6] = v;
  __syncthreads();
  float s = 0;
  for (int i = 0; i < (int)(blockDim.x >> 6); ++i) s += red[i];
  return s;
}

__device__ void ln576(float* xs, float* red) {
  float s = 0;
  for (int i = threadIdx.x; i < D; i += blockDim.x) s += xs[i];
  float m = block_sum(s, red) / D;
  float v = 0;
  for (int i = threadIdx.x; i < D; i += blockDim.x) { float d = xs[i] - m; v += d * d; }
  v = block_sum(v, red) / D;
  float mm = mr(m), vv = mr(v);
  float den = mr(sqrtf(mr(vv + 1e-5f)));
  for (int i = threadIdx.x; i < D; i += blockDim.x) xs[i] = mr(mr(xs[i] - mm) / den);
  __syncthreads();
}

__global__ void k_q(const u16* x, const u16* wq, float* q) {
  __shared__ float xs[D]; __shared__ float red[16]; __shared__ float part[4][64];
  for (int i = threadIdx.x; i < D; i += blockDim.x) xs[i] = bf2f(x[i]);
  __syncthreads();
  ln576(xs, red);
  int c = blockIdx.x * 64 + (threadIdx.x & 63), g = threadIdx.x >> 6;
  float a = 0;
  for (int r = g; r < D; r += 4) a += xs[r] * bf2f(wq[(size_t)r * (H * DH) + c]);
  part[g][threadIdx.x & 63] = a;
  __syncthreads();
  if (g == 0) q[c] = mr(part[0][threadIdx.x] + part[1][threadIdx.x] + part[2][threadIdx.x] + part[3][threadIdx.x]);
}

__global__ void __launch_bounds__(256) k_attn(const float* q, const u16* K, const u16* V, float* part) {
  const int s = blockIdx.x, h = blockIdx.y;
  const int lane = threadIdx.x & 63, w = threadIdx.x >> 6, r = lane >> 4, c = lane & 15;
  constexpr int KPS = SK / NSPLIT;
  float qf[8];
  for (int j = 0; j < 8; ++j) qf[j] = q[h * DH + 8 * c + j];
  const u16* kb = K + ((size_t)h * SK + (size_t)s * KPS) * DH + 8 * c;
  const u16* vb = V + ((size_t)h * SK + (size_t)s * KPS) * DH + 8 * c;
  float m = -INFINITY, l = 0, acc[8] = {0};
  for (int k0 = w * 16; k0 < KPS; k0 += 64) {
    u32x4 kk[4], vv[4];
    for (int t = 0; t < 4; ++t) {
      kk[t] = __builtin_nontemporal_load((const u32x4*)(kb + (size_t)(k0 + 4 * t + r) * DH));
      vv[t] = __builtin_nontemporal_load((const u32x4*)(vb + (size_t)(k0 + 4 * t + r) * DH));
    }
    float sc[4];
    for (int t = 0; t < 4; ++t) {
      float d = 0;
      for (int j = 0; j < 4; ++j) { d += lo(kk[t][j]) * qf[2 * j]; d += hi(kk[t][j]) * qf[2 * j + 1]; }
      d += __shfl_xor(d, 1); d += __shfl_xor(d, 2); d += __shfl_xor(d, 4); d += __shfl_xor(d, 8);
      sc[t] = mr(mr(d) * 0.08838834764831845f);
    }
    float mn = fmaxf(fmaxf(fmaxf(sc[0], sc[1]), fmaxf(sc[2], sc[3])), m);
    float al = __expf(m - mn);
    l *= al;
    for (int j = 0; j < 8; ++j) acc[j] *= al;
    for (int t = 0; t < 4; ++t) {
      float p = __expf(sc[t] - mn);
      l += p;
      for (int j = 0; j < 4; ++j) { acc[2 * j] += p * lo(vv[t][j]); acc[2 * j + 1] += p * hi(vv[t][j]); }
    }
    m = mn;
  }
  __shared__ float sm[16], sl[16], sa[16][DH];
  int g = w * 4 + r;
  if (c == 0) { sm[g] = m; sl[g] = l; }
  for (int j = 0; j < 8; ++j) sa[g][8 * c + j] = acc[j];
  __syncthreads();
  if (threadIdx.x < DH) {
    float M = -INFINITY;
    for (int i = 0; i < 16; ++i) M = fmaxf(M, sm[i]);
    float L = 0, o = 0;
    for (int i = 0; i < 16; ++i) { float e = __expf(sm[i] - M); L += e * sl[i]; o += e * sa[i][threadIdx.x]; }
    float* p = part + ((size_t)h * NSPLIT + s) * (DH + 2);
    p[2 + threadIdx.x] = o;
    if (threadIdx.x == 0) { p[0] = M; p[1] = L; }
  }
}

__global__ void k_wo(const float* part, const u16* wo, float* y) {
  __shared__ float o[DH];
  int h = blockIdx.x;
  if (threadIdx.x < DH) {
    float M = -INFINITY;
    for (int s = 0; s < NSPLIT; ++s) M = fmaxf(M, part[((size_t)h * NSPLIT + s) * (DH + 2)]);
    float L = 0, a = 0;
    for (int s = 0; s < NSPLIT; ++s) { const float* p = part + ((size_t)h * NSPLIT + s) * (DH + 2); float e = __expf(p[0] - M); L += e * p[1]; a += e * p[2 + threadIdx.x]; }
    o[threadIdx.x] = mr(a / L);
  }
  __syncthreads();
  float a = 0;
  for (int d = 0; d < DH; ++d) a += o[d] * bf2f(wo[(size_t)(h * DH + d) * D + threadIdx.x]);
  y[h * D + threadIdx.x] = a;
}

__device__ __forceinline__ float gelu_tanh(float x) {
  float u = 0.7978845608028654f * (x + 0.044715f * x * x * x);
  return 0.5f * x * (1.f + tanhf(u));
}

__global__ void k_gu(const u16* x, const float* y, const u16* wg, const u16* wu, float* act, float* hout) {
  __shared__ float xs[D]; __shared__ float red[16]; __shared__ float pg[4][64], pu[4][64];
  for (int i = threadIdx.x; i < D; i += blockDim.x) {
    float a = 0;
    for (int h = 0; h < H; ++h) a += y[h * D + i];
    float hv = mr(bf2f(x[i]) + mr(a));
    xs[i] = hv;
    if (blockIdx.x == 0) hout[i] = hv;
  }
  __syncthreads();
  ln576(xs, red);
  int c = blockIdx.x * 64 + (threadIdx.x & 63), g = threadIdx.x >> 6;
  float a = 0, b = 0;
  for (int r = g; r < D; r += 4) { a += xs[r] * bf2f(wg[(size_t)r * DF + c]); b += xs[r] * bf2f(wu[(size_t)r * DF + c]); }
  pg[g][threadIdx.x & 63] = a; pu[g][threadIdx.x & 63] = b;
  __syncthreads();
  if (g == 0) {
    int t = threadIdx.x;
    float gg = mr(pg[0][t] + pg[1][t] + pg[2][t] + pg[3][t]);
    float uu = mr(pu[0][t] + pu[1][t] + pu[2][t] + pu[3][t]);
    act[c] = mr(mr(gelu_tanh(gg)) * uu);
  }
}

__global__ void k_dn(const float* act, const u16* wd, float* z) {
  float a = 0;
  for (int r = blockIdx.x * 64; r < blockIdx.x * 64 + 64; ++r) a += act[r] * bf2f(wd[(size_t)r * D + threadIdx.x]);
  z[blockIdx.x * D + threadIdx.x] = a;
}
__global__ void k_fin(const float* z, const float* hv, __hip_bfloat16* out) {
  float a = 0;
  for (int i = 0; i < 72; ++i) a += z[i * D + threadIdx.x];
  out[threadIdx.x] = __float2bfloat16(hv[threadIdx.x] + mr(a));
}

extern "C" void kernel_launch(void* const* d_in, const int* in_sizes, int n_in, void* d_out, int out_size, void* d_ws, size_t ws_size,
                              hipStream_t stream) {
  const u16* x = (const u16*)d_in[0]; const u16* K = (const u16*)d_in[1]; const u16* V = (const u16*)d_in[2];
  const u16* wq = (const u16*)d_in[3]; const u16* wo = (const u16*)d_in[4];
  const u16* wg = (const u16*)d_in[6]; const u16* wu = (const u16*)d_in[7]; const u16* wd = (const u16*)d_in[8];
  float* ws = (float*)d_ws;
  float* q = ws; float* part = q + 4096; float* y = part + H * NSPLIT * (DH + 2); float* act = y + H * D; float* hv = act + DF; float* z = hv + D;
  k_q<<<64, 256, 0, stream>>>(x, wq, q);
  k_attn<<<dim3(NSPLIT, H), 256, 0, stream>>>(q, K, V, part);
  k_wo<<<H, D, 0, stream>>>(part, wo, y);
  k_gu<<<72, 256, 0, stream>>>(x, y, wg, wu, act, hv);
  k_dn<<<72, D, 0, stream>>>(act, wd, z);
  k_fin<<<1, D, 0, stream>>>(z, hv, (__hip_bfloat16*)d_out);
}
```

```cpp
#define AB_WARM 2
#define AB_GUDEF 1
#include <hip/hip_runtime.h>
#include <stdint.h>

typedef unsigned short u16;
typedef unsigned int u32;
typedef unsigned long long u64;
typedef u32 u32x2 __attribute__((ext_vector_type(2)));
typedef u32 u32x3 __attribute__((ext_vector_type(3)));
typedef u32 u32x4 __attribute__((ext_vector_type(4)));
typedef float f32x2 __attribute__((ext_vector_type(2)));
typedef float f32x4 __attribute__((ext_vector_type(4)));
typedef __bf16 bf16x2 __attribute__((ext_vector_type(2)));
#define GAS __attribute__((address_space(1)))
#define LAS __attribute__((address_space(3)))
#define RLX_AGENT __ATOMIC_RELAXED, __HIP_MEMORY_SCOPE_AGENT

#define STAMP 0

#define PH_T0 450
#define PH_DT 300
constexpr int D = 576, NH = 32, SK = 16384, DH = 128, DF = 4608;
constexpr int NWG = 256, NTHR = 1024;
constexpr u32 SENT = 0xFFFFFFFFu;
constexpr u32 SPIN_CAP = 1u << 18;
constexpr u32 OFF_EPOCH = 0, OFF_FLAGS = 4096, OFF_ML = 8192, OFF_H = 16384, OFF_Y1 = 32768, YBYTES = NWG * D * 4,
              OFF_Y3 = OFF_Y1 + YBYTES, OFF_TS = OFF_Y3 + YBYTES;
constexpr int L_QPART = 0, L_QX = L_QPART + 9 * 128, L_SC = L_QX + 4 * 64, L_PART = L_SC + 4 * 8 * 64, L_FW = L_PART + 4 * 128,
              L_ML = L_FW + 4, L_HN = L_ML + 8, L_ACT = L_HN + 5 * 64, L_GU = L_ACT + 12, L_END = L_GU + 2 * 5184;

__device__ __forceinline__ float asf(u32 v) { return __builtin_bit_cast(float, v); }
__device__ __forceinline__ u32 asu(float v) { return __builtin_bit_cast(u32, v); }
__device__ __forceinline__ float dot2(u32 a, u32 b, float c) {
  return __builtin_amdgcn_fdot2_f32_bf16(__builtin_bit_cast(bf16x2, a), __builtin_bit_cast(bf16x2, b), c, false);
}
__device__ __forceinline__ u32 pk2(float lo, float hi) {
  bf16x2 r = {(__bf16)lo, (__bf16)hi};
  return __builtin_bit_cast(u32, r);
}
__device__ __forceinline__ float rbf(float f) { return asf(pk2(f, f) << 16); }
template <int CTRL> __device__ __forceinline__ float dpp(float x) {
  return asf((u32)__builtin_amdgcn_mov_dpp((int)asu(x), CTRL, 0xf, 0xf, true));
}
template <int CTRL> __device__ __forceinline__ u32 dppu(u32 x) { return (u32)__builtin_amdgcn_mov_dpp((int)x, CTRL, 0xf, 0xf, true); }
constexpr int XOR1 = 0xB1, XOR2 = 0x4E, HMIR = 0x141, ROR8 = 0x128, ROR4 = 0x124, ROR2 = 0x122, ROR1 = 0x121, BCAST = 0x150;
__device__ __forceinline__ float sw32add(float a, float b) {
  auto s = __builtin_amdgcn_permlane32_swap(asu(a), asu(b), false, false);
  return asf(s[0]) + asf(s[1]);
}
__device__ __forceinline__ float sw16add(float a, float b) {
  auto s = __builtin_amdgcn_permlane16_swap(asu(a), asu(b), false, false);
  return asf(s[0]) + asf(s[1]);
}
__device__ __forceinline__ float row_sum(float v) {
  v += dpp<ROR8>(v); v += dpp<ROR4>(v); v += dpp<ROR2>(v); v += dpp<ROR1>(v);
  return v;
}
__device__ __forceinline__ float wave_sum(float v) { v = row_sum(v); v = sw16add(v, v); return sw32add(v, v); }
__device__ __forceinline__ float wave_max(float v) {
  v = fmaxf(v, dpp<ROR8>(v)); v = fmaxf(v, dpp<ROR4>(v)); v = fmaxf(v, dpp<ROR2>(v)); v = fmaxf(v, dpp<ROR1>(v));
  auto s = __builtin_amdgcn_permlane16_swap(asu(v), asu(v), false, false);
  v = fmaxf(asf(s[0]), asf(s[1]));
  auto t = __builtin_amdgcn_permlane32_swap(asu(v), asu(v), false, false);
  return fmaxf(asf(t[0]), asf(t[1]));
}
__device__ __forceinline__ u32 canon(float v) { const u32 u = asu(v); return u == SENT ? 0x7FC00000u : u; }
__device__ __forceinline__ void st_sc1(GAS unsigned char* p, u32 v) { __hip_atomic_store((GAS u32*)p, v, RLX_AGENT); }
__device__ __forceinline__ u32 ld_sc1(GAS unsigned char* p) { return __hip_atomic_load((GAS u32*)p, RLX_AGENT); }
__device__ __forceinline__ void bar() {
  asm volatile("s_waitcnt lgkmcnt(0)" ::: "memory");
  __builtin_amdgcn_s_barrier();
  asm volatile("" ::: "memory");
}
#define ORDER() do { asm volatile("" ::: "memory"); __builtin_amdgcn_sched_barrier(0); } while (0)
__device__ __forceinline__ void ln_stats(const float (&v)[9], float& mb, float& rden) {
  float s = ((v[0] + v[1]) + (v[2] + v[3])) + ((v[4] + v[5]) + (v[6] + v[7])) + v[8];
  const float mean = wave_sum(s) * (1.0f / D);
  float q = 0.f;
#pragma unroll
  for (int i = 0; i < 9; ++i) { const float d = v[i] - mean; q = fmaf(d, d, q); }
  const float var = wave_sum(q) * (1.0f / D);
  mb = rbf(mean);
  const float den = rbf(__builtin_amdgcn_sqrtf(rbf(rbf(var) + 1e-5f)));
  rden = __builtin_amdgcn_rcpf(den);
}
#define TS(k) do {} while (0)

__device__ __forceinline__ float gather_sum(GAS unsigned char* base, int col0, int lane) {
  const __amdgpu_buffer_rsrc_t rs = __builtin_amdgcn_make_buffer_rsrc((void*)base, 0, YBYTES, 0x00020000);
  u32 off[4];
  u32x4 v[4];
#pragma unroll
  for (int k = 0; k < 4; ++k) { off[k] = (u32)(lane + 64 * k) * (D * 4) + col0 * 4; v[k] = u32x4{0u, 0u, 0u, 0u}; }
#pragma unroll 1
  for (u32 spin = 0; spin < SPIN_CAP; ++spin) {
    u32x4 n[4];
#pragma unroll
    for (int k = 0; k < 4; ++k) n[k] = __builtin_amdgcn_raw_buffer_load_b128(rs, off[k], 0, 16);
    __builtin_amdgcn_sched_barrier(0);
    bool pend = false;
#pragma unroll
    for (int k = 0; k < 4; ++k) {
      const bool live = off[k] != 0x80000000u;
      const bool bad = n[k][0] == SENT || n[k][1] == SENT || n[k][2] == SENT || n[k][3] == SENT;
      if (live) v[k] = n[k];
      if (!bad) off[k] = 0x80000000u;
      pend |= bad;
    }
    if (!__any(pend)) break;
    asm volatile("s_sleep 1" ::: "memory");
  }
  float s[4];
#pragma unroll
  for (int e = 0; e < 4; ++e) s[e] = (asf(v[0][e]) + asf(v[1][e])) + (asf(v[2][e]) + asf(v[3][e]));
  const float z0 = sw32add(s[0], s[2]), z1 = sw32add(s[1], s[3]);
  return row_sum(sw16add(z0, z1));
}

__global__ void __launch_bounds__(NTHR) fused_decode(const u16* x, const u16* Kc, const u16* Vc,
                                                     const u16* wq, const u16* wo, const u16* wg,
                                                     const u16* wu, const u16* wd, u16* out,
                                                     unsigned char* ws_) {
  __shared__ __attribute__((aligned(16))) u32 lds[L_END];
  GAS unsigned char* ws = (GAS unsigned char*)ws_;
  const int lane = threadIdx.x & 63, wave = __builtin_amdgcn_readfirstlane(threadIdx.x >> 6);
  const int r = lane >> 4, c = lane & 15;
  const int b0 = blockIdx.x, xg = b0 & 7, idx = b0 >> 3, chunk = xg * 32 + idx, head = chunk >> 3;

  if (wave < 4) {
    const char* kb = (const char*)Kc + (size_t)chunk * 524288 + wave * 4096;
    const char* vb = (const char*)Vc + (size_t)chunk * 524288 + wave * 4096;
    const u32 lo = lane * 16;
    const int rot = b0 * 37;
    u32x4 buf[16];
#define ISSUE4(base, n, bq) do { const char* p_ = (base) + (size_t)((((n) + rot) & 31) * 16384); \
    _Pragma("unroll") for (int t_ = 0; t_ < 4; ++t_) buf[(bq) * 4 + t_] = __builtin_nontemporal_load((const u32x4*)(p_ + lo + t_ * 1024)); } while (0)
    TS(0);
    bar();
#pragma unroll
    for (int bq = 0; bq < 4; ++bq) ISSUE4(kb, bq, bq);
    bar();
    u32x4 q;
    {
      f32x2 a = {0.f, 0.f};
#pragma unroll
      for (int j = 0; j < 9; ++j) a += *(const f32x2*)&lds[L_QPART + j * 128 + 2 * lane];
      lds[L_QX + wave * 64 + lane] = pk2(a[0], a[1]);
      asm volatile("s_waitcnt lgkmcnt(0)" ::: "memory");
      q = *(const u32x4*)&lds[L_QX + wave * 64 + 4 * c];
    }
    if (wave == 0) TS(1);
    u32* sc = &lds[L_SC + wave * 512 + lane];
#pragma unroll 1
    for (int g = 0; g < 8; ++g) {
      const char* nb = g < 7 ? kb : vb;
      const int nn = (4 * g + 4) & 31;
      float sreg = 0.f;
#pragma unroll
      for (int bq = 0; bq < 4; ++bq) {
        float xs[4];
#pragma unroll
        for (int t = 0; t < 4; ++t) {
          const u32x4 k = buf[bq * 4 + t];
          xs[t] = dot2(k[3], q[3], dot2(k[2], q[2], dot2(k[1], q[1], dot2(k[0], q[0], 0.f))));
        }
        asm volatile("" : "+v"(xs[0]), "+v"(xs[1]), "+v"(xs[2]), "+v"(xs[3]));
        ISSUE4(nb, nn + bq, bq);
        __builtin_amdgcn_sched_barrier(0);
        const float a0 = xs[0] + dpp<ROR8>(xs[0]), a1 = xs[1] + dpp<ROR8>(xs[1]), a2 = xs[2] + dpp<ROR8>(xs[2]), a3 = xs[3] + dpp<ROR8>(xs[3]);
        const float y0 = (c & 8) ? a2 : a0, y1 = (c & 8) ? a3 : a1;
        const float e0 = y0 + dpp<HMIR>(y0), e1 = y1 + dpp<HMIR>(y1);
        float z = (c & 4) ? e1 : e0;
        z += dpp<XOR2>(z); z += dpp<XOR1>(z);
        sreg = ((c & 3) == bq) ? z : sreg;
      }
      sc[g * 64] = asu(rbf(rbf(sreg) * 0.08838834764831845f));
    }
    if (wave == 0) TS(2);
    {
      float s[8];
#pragma unroll
      for (int g = 0; g < 8; ++g) s[g] = asf(sc[g * 64]);
      const float m = wave_max(fmaxf(fmaxf(fmaxf(s[0], s[1]), fmaxf(s[2], s[3])), fmaxf(fmaxf(s[4], s[5]), fmaxf(s[6], s[7]))));
      float l = 0.f;
#pragma unroll
      for (int g = 0; g < 8; ++g) { const float p = __builtin_amdgcn_exp2f((s[g] - m) * 1.4426950408889634f); l += p; sc[g * 64] = asu(p); }
      l = wave_sum(l);
      if (lane == 0) *(volatile LAS u64*)&lds[L_ML + 2 * wave] = ((u64)canon(l) << 32) | canon(m);
    }
    float acc[8];
#pragma unroll
    for (int k = 0; k < 8; ++k) acc[k] = 0.f;
#define PV4(bq, pg) do { \
    _Pragma("unroll") for (int t_ = 0; t_ < 4; ++t_) { \
      const float pb_ = t_ == 0 ? dpp<BCAST + (bq)>(pg) : t_ == 1 ? dpp<BCAST + 4 + (bq)>(pg) : t_ == 2 ? dpp<BCAST + 8 + (bq)>(pg) : dpp<BCAST + 12 + (bq)>(pg); \
      const u32x4 v_ = buf[(bq) * 4 + t_]; \
      _Pragma("unroll") for (int e_ = 0; e_ < 4; ++e_) { \
        acc[2 * e_] = fmaf(pb_, asf(v_[e_] << 16), acc[2 * e_]); acc[2 * e_ + 1] = fmaf(pb_, asf(v_[e_] & 0xffff0000u), acc[2 * e_ + 1]); } } \
    asm volatile("" : "+v"(acc[0]), "+v"(acc[1]), "+v"(acc[2]), "+v"(acc[3]), "+v"(acc[4]), "+v"(acc[5]), "+v"(acc[6]), "+v"(acc[7])); } while (0)
#pragma unroll 1
    for (int g = 0; g < 7; ++g) {
      const float pg = asf(sc[g * 64]);
      const int nn = 4 * g + 4;
      PV4(0, pg); ISSUE4(vb, nn + 0, 0); __builtin_amdgcn_sched_barrier(0);
      PV4(1, pg); ISSUE4(vb, nn + 1, 1); __builtin_amdgcn_sched_barrier(0);
      PV4(2, pg); ISSUE4(vb, nn + 2, 2); __builtin_amdgcn_sched_barrier(0);
      PV4(3, pg); ISSUE4(vb, nn + 3, 3); __builtin_amdgcn_sched_barrier(0);
    }
    {
      const float pg = asf(sc[7 * 64]);
      PV4(0, pg); PV4(1, pg); PV4(2, pg); PV4(3, pg);
    }
    const float b0_ = sw32add(acc[0], acc[4]), b1_ = sw32add(acc[1], acc[5]), b2_ = sw32add(acc[2], acc[6]), b3_ = sw32add(acc[3], acc[7]);
    const f32x2 e = {sw16add(b0_, b2_), sw16add(b1_, b3_)};
    *(f32x2*)&lds[L_PART + wave * 128 + 8 * c + 2 * r] = e;
    TS(4 + wave);
    bar();
    return;
  }

  if (wave < 13) {
    const int j = wave - 4;
    __builtin_amdgcn_s_setprio(2);
    const u64 tstart = __builtin_amdgcn_s_memrealtime();
    u16 xr[9];
#pragma unroll
    for (int i = 0; i < 9; ++i) xr[i] = x[lane + 64 * i];
    const u16 xo = x[64 * j + lane];
    ORDER();
    u32x4 wqv[16];
    {
      const __amdgpu_buffer_rsrc_t rq = __builtin_amdgcn_make_buffer_rsrc((void*)wq, 0, D * NH * DH * 2, 0x00020000);
      const u32 vo = r * (16 * 8192) + c * 16;
      const u32 so = (64 * j) * 8192 + head * 256;
#pragma unroll
      for (int i = 0; i < 16; ++i) {
        wqv[i] = __builtin_amdgcn_raw_buffer_load_b128(rq, vo, so + i * 8192, 2);
        ORDER();
      }
    }
    bar();
    u16 wdr[18];
    {
      const u16* p = wd + (size_t)(18 * chunk) * D + 64 * j + lane;
#pragma unroll
      for (int k = 0; k < 18; ++k)
        wdr[k] = __builtin_nontemporal_load(p + k * D);
    }
    ORDER();
    {
      float xv[9];
#pragma unroll
      for (int i = 0; i < 9; ++i) xv[i] = asf((u32)xr[i] << 16);
      const float xown = asf((u32)xo << 16);
      float mb, rden;
      ln_stats(xv, mb, rden);
      const u32 xh = asu(rbf(rbf(xown - mb) * rden));
      const u32 xl = xh >> 16;
      float qa[8];
#pragma unroll
      for (int k = 0; k < 8; ++k) qa[k] = 0.f;
#define QROW(i) do { const u32 l_ = dppu<BCAST + (i)>(xl), h_ = dppu<BCAST + (i)>(xh); \
      _Pragma("unroll") for (int e_ = 0; e_ < 4; ++e_) { qa[2 * e_] = dot2(wqv[i][e_], l_, qa[2 * e_]); qa[2 * e_ + 1] = dot2(wqv[i][e_], h_, qa[2 * e_ + 1]); } } while (0)
      QROW(0); QROW(1); QROW(2); QROW(3); QROW(4); QROW(5); QROW(6); QROW(7);
      QROW(8); QROW(9); QROW(10); QROW(11); QROW(12); QROW(13); QROW(14); QROW(15);
      const float b0_ = sw32add(qa[0], qa[4]), b1_ = sw32add(qa[1], qa[5]), b2_ = sw32add(qa[2], qa[6]), b3_ = sw32add(qa[3], qa[7]);
      const f32x2 e = {sw16add(b0_, b2_), sw16add(b1_, b3_)};
      *(f32x2*)&lds[L_QPART + j * 128 + 8 * c + 2 * r] = e;
    }
    bar();
    ORDER();
    u32 wdv[9];
#pragma unroll
    for (int k = 0; k < 9; ++k) { wdv[k] = (u32)wdr[2 * k] | ((u32)wdr[2 * k + 1] << 16); asm volatile("" : "+v"(wdv[k])); }
    {
      const u64 tgo = tstart + (u64)(PH_T0 + PH_DT * j);
#pragma unroll 1
      while (__builtin_amdgcn_s_memrealtime() < tgo) __builtin_amdgcn_s_sleep(16);
    }
    ORDER();
    u32 tch[2];
    {
#pragma unroll
      for (int k = 0; k < 2; ++k) {
        u32 p = k * 64 + lane; p = p < 72 ? p : 71;
        const u32 seg = p / 18, pc = p - seg * 18;
        const u32* a_ = (const u32*)((const char*)((seg & 1) ? wu : wg) + (64 * j + 2 * idx + (seg >> 1)) * (DF * 2) + xg * 1152 + pc * 64);
        tch[k] = AB_WARM == 1 ? __builtin_nontemporal_load(a_) : *a_;
      }
    }
    ORDER();
    u32x2 wov[32];
    {
      const char* p = (const char*)wo + (size_t)(head * 128) * (D * 2) + j * 128;
      const u32 vo = r * (32 * D * 2) + c * 8;
#pragma unroll
      for (int i = 0; i < 32; ++i)
        wov[i] = __builtin_nontemporal_load((const u32x2*)(p + i * (D * 2) + vo));
    }
    ORDER();
    asm volatile("" :: "v"(tch[0]), "v"(tch[1]));
    u32 W[16][4];
#pragma unroll
    for (int pi = 0; pi < 16; ++pi) {
      const u32x2 A = wov[2 * pi], B = wov[2 * pi + 1];
      W[pi][0] = __builtin_amdgcn_perm(B[0], A[0], 0x05040100); W[pi][1] = __builtin_amdgcn_perm(B[0], A[0], 0x07060302);
      W[pi][2] = __builtin_amdgcn_perm(B[1], A[1], 0x05040100); W[pi][3] = __builtin_amdgcn_perm(B[1], A[1], 0x07060302);
      asm volatile("" : "+v"(W[pi][0]), "+v"(W[pi][1]), "+v"(W[pi][2]), "+v"(W[pi][3]));
    }
    ORDER();
    {
      u32x3 gv[3], uv[3];
#pragma unroll
      for (int k = 0; k < 3; ++k) {
        const u32 p = (j * 3 + k) * 64 + lane, row = p / 3, th = p - row * 3;
        const u32 o = row * (DF * 2) + chunk * 36 + th * 12;
        gv[k] = __builtin_amdgcn_raw_buffer_load_b96(__builtin_amdgcn_make_buffer_rsrc((void*)wg, 0, D * DF * 2, 0x00020000), o, 0, 0);
        uv[k] = __builtin_amdgcn_raw_buffer_load_b96(__builtin_amdgcn_make_buffer_rsrc((void*)wu, 0, D * DF * 2, 0x00020000), o, 0, 0);
      }
#pragma unroll
      for (int k = 0; k < 3; ++k) {
        const int p = (j * 3 + k) * 64 + lane;
#pragma unroll
        for (int e = 0; e < 3; ++e) { lds[L_GU + p * 3 + e] = gv[k][e]; lds[L_GU + 5184 + p * 3 + e] = uv[k][e]; }
      }
    }
    if (j == 8) TS(3);
    bar();
    if (j == 0) TS(8);
    {
      const f32x4 f = *(const f32x4*)&lds[L_FW];
      f32x2 a = f[0] * *(const f32x2*)&lds[L_PART + 2 * lane];
      a += f[1] * *(const f32x2*)&lds[L_PART + 128 + 2 * lane];
      a += f[2] * *(const f32x2*)&lds[L_PART + 256 + 2 * lane];
      a += f[3] * *(const f32x2*)&lds[L_PART + 384 + 2 * lane];
      const u32 ap = pk2(a[0], a[1]);
      float y[4] = {0.f, 0.f, 0.f, 0.f};
#define WOROW(pi) do { const u32 a_ = dppu<BCAST + (pi)>(ap); \
      _Pragma("unroll") for (int e_ = 0; e_ < 4; ++e_) y[e_] = dot2(W[pi][e_], a_, y[e_]); } while (0)
      WOROW(0); WOROW(1); WOROW(2); WOROW(3); WOROW(4); WOROW(5); WOROW(6); WOROW(7);
      WOROW(8); WOROW(9); WOROW(10); WOROW(11); WOROW(12); WOROW(13); WOROW(14); WOROW(15);
      const float yy = sw16add(sw32add(y[0], y[2]), sw32add(y[1], y[3]));
      st_sc1(ws + OFF_Y1 + chunk * (D * 4) + (64 * j + 4 * c + r) * 4, canon(yy));
    }
    if (j == 0) TS(9);
    __builtin_amdgcn_sched_barrier(0);
    u32 G[5][2], U[5][2];
    {
      u32 gw[9], uw[9];
#pragma unroll
      for (int i = 0; i < 9; ++i) { gw[i] = lds[L_GU + (lane + 64 * i) * 9 + j]; uw[i] = lds[L_GU + 5184 + (lane + 64 * i) * 9 + j]; }
#pragma unroll
      for (int k = 0; k < 4; ++k) {
        G[k][0] = __builtin_amdgcn_perm(gw[2 * k + 1], gw[2 * k], 0x05040100); G[k][1] = __builtin_amdgcn_perm(gw[2 * k + 1], gw[2 * k], 0x07060302);
        U[k][0] = __builtin_amdgcn_perm(uw[2 * k + 1], uw[2 * k], 0x05040100); U[k][1] = __builtin_amdgcn_perm(uw[2 * k + 1], uw[2 * k], 0x07060302);
      }
      G[4][0] = gw[8] & 0xffffu; G[4][1] = gw[8] >> 16; U[4][0] = uw[8] & 0xffffu; U[4][1] = uw[8] >> 16;
#pragma unroll
      for (int k = 0; k < 5; ++k) asm volatile("" : "+v"(G[k][0]), "+v"(G[k][1]), "+v"(U[k][0]), "+v"(U[k][1]));
    }
    bar();
    if (j == 0) TS(12);
    {
      float g0 = 0.f, g1 = 0.f, u0 = 0.f, u1 = 0.f;
#pragma unroll
      for (int k = 0; k < 5; ++k) {
        const u32 hp = lds[L_HN + k * 64 + lane];
        g0 = dot2(G[k][0], hp, g0); g1 = dot2(G[k][1], hp, g1); u0 = dot2(U[k][0], hp, u0); u1 = dot2(U[k][1], hp, u1);
      }
      const float v = rbf(row_sum(sw16add(sw32add(g0, g1), sw32add(u0, u1))));
      auto s = __builtin_amdgcn_permlane16_swap(asu(v), asu(v), false, false);
      const float gg = asf(s[0]), uu = asf(s[1]);
      const float t = gg * fmaf(gg * gg, 0.044715f, 1.0f) * (-2.0f * 0.7978845608028654f * 1.4426950408889634f);
      const float ge = rbf(gg * __builtin_amdgcn_rcpf(1.0f + __builtin_amdgcn_exp2f(t)));
      const u32 a = pk2(ge * uu, 0.f);
      if ((lane & 31) == 0) ((u16*)&lds[L_ACT])[2 * j + (lane >> 5)] = (u16)a;
    }
    bar();
    {
      const u32x4 a0 = *(const u32x4*)&lds[L_ACT], a1 = *(const u32x4*)&lds[L_ACT + 4];
      const u32 a2 = lds[L_ACT + 8];
      float y0 = dot2(wdv[0], a0[0], 0.f), y1 = dot2(wdv[1], a0[1], 0.f);
      y0 = dot2(wdv[2], a0[2], y0); y1 = dot2(wdv[3], a0[3], y1);
      y0 = dot2(wdv[4], a1[0], y0); y1 = dot2(wdv[5], a1[1], y1);
      y0 = dot2(wdv[6], a1[2], y0); y1 = dot2(wdv[7], a1[3], y1);
      y0 = dot2(wdv[8], a2, y0);
      st_sc1(ws + OFF_Y3 + chunk * (D * 4) + (64 * j + lane) * 4, canon(y0 + y1));
    }
    if (j == 0) TS(13);
    return;
  }

  __builtin_amdgcn_s_setprio(3);
  const int line = xg + 8 * (idx >> 3), col0 = 32 * line + 4 * (idx & 7);
  if (wave == 13) {
    const bool act = line < 18;
    u64 ep = 0;
    bar();
    bar();
    const float xv = act ? asf((u32)x[col0 + r] << 16) : 0.f;
    if (b0 == 0) ep = __hip_atomic_load((GAS u64*)(ws + OFF_EPOCH), RLX_AGENT);
    bar();
    if (!act) return;
    const float hv = rbf(xv + rbf(gather_sum(ws + OFF_Y1, col0, lane)));
    if (c == 0) st_sc1(ws + OFF_H + (col0 + r) * 4, canon(hv));
    TS(10);
    bar();
    bar();
    const float o = hv + rbf(gather_sum(ws + OFF_Y3, col0, lane));
    if (c == 0) out[col0 + r] = (u16)pk2(o, 0.f);
    if (b0 == 0 && lane == 0) *(GAS u64*)(ws + OFF_EPOCH) = ep * 0x9E3779B97F4A7C15ull + 0x632BE59BD9B4E019ull;
    TS(14);
    return;
  }

  if (wave == 14) {
    bar();
    bar();
    bar();
    float hv[9];
#pragma unroll 1
    for (u32 spin = 0; spin < SPIN_CAP; ++spin) {
      bool bad = false;
#pragma unroll
      for (int i = 0; i < 9; ++i) { const u32 u = ld_sc1(ws + OFF_H + (lane + 64 * i) * 4); hv[i] = asf(u); bad |= u == SENT; }
      if (!__any(bad)) break;
      asm volatile("s_sleep 1" ::: "memory");
    }
    TS(11);
    float mb, rden;
    ln_stats(hv, mb, rden);
#pragma unroll
    for (int i = 0; i < 9; ++i) hv[i] = rbf(hv[i] - mb) * rden;
#pragma unroll
    for (int k = 0; k < 4; ++k) lds[L_HN + k * 64 + lane] = pk2(hv[2 * k], hv[2 * k + 1]);
    lds[L_HN + 256 + lane] = pk2(hv[8], 0.f);
    bar();
    return;
  }

  {
    if (lane < 8) lds[L_ML + lane] = SENT;
    bar();
    bar();
    const u64 ep = __hip_atomic_load((GAS u64*)(ws + OFF_EPOCH), RLX_AGENT);
    {
      const __amdgpu_buffer_rsrc_t r1 = __builtin_amdgcn_make_buffer_rsrc((void*)(ws + OFF_Y1 + chunk * (D * 4)), 0, D * 4, 0x00020000);
      const __amdgpu_buffer_rsrc_t r3 = __builtin_amdgcn_make_buffer_rsrc((void*)(ws + OFF_Y3 + chunk * (D * 4)), 0, D * 4, 0x00020000);
      const u32x4 s4 = {SENT, SENT, SENT, SENT};
#pragma unroll
      for (int k = 0; k < 3; ++k) {
        __builtin_amdgcn_raw_buffer_store_b128(s4, r1, k * 1024 + lane * 16, 0, 16);
        __builtin_amdgcn_raw_buffer_store_b128(s4, r3, k * 1024 + lane * 16, 0, 16);
      }
      if (lane < 8) st_sc1(ws + OFF_ML + chunk * 32 + lane * 4, SENT);
      if (lane < 4 && line < 18) st_sc1(ws + OFF_H + (col0 + lane) * 4, SENT);
    }
    asm volatile("s_waitcnt vmcnt(0)" ::: "memory");
    const u64 tag = ep * 0x9E3779B97F4A7C15ull + 0x632BE59BD9B4E019ull;
    if (lane == 0) __hip_atomic_store((GAS u64*)(ws + OFF_FLAGS + b0 * 8), tag, RLX_AGENT);
#pragma unroll 1
    for (u32 spin = 0; spin < SPIN_CAP; ++spin) {
      asm volatile("s_sleep 64" ::: "memory");
      bool bad = false;
#pragma unroll
      for (int k = 0; k < 4; ++k) bad |= __hip_atomic_load((GAS u64*)(ws + OFF_FLAGS + (lane + 64 * k) * 8), RLX_AGENT) != tag;
      if (!__any(bad)) break;
    }
#pragma unroll 1
    for (u32 spin = 0; spin < SPIN_CAP; ++spin) {
      asm volatile("s_sleep 32" ::: "memory");
      const u64 v = *(volatile LAS u64*)&lds[L_ML + 2 * (lane & 3)];
      if (!__any((u32)v == SENT || (u32)(v >> 32) == SENT)) {
        if (lane < 4) __hip_atomic_store((GAS u64*)(ws + OFF_ML + (chunk * 4 + lane) * 8), v, RLX_AGENT);
        break;
      }
    }
    float m, l;
#pragma unroll 1
    for (u32 spin = 0; spin < SPIN_CAP; ++spin) {
      asm volatile("s_sleep 64" ::: "memory");
      const u64 v = __hip_atomic_load((GAS u64*)(ws + OFF_ML + head * 256 + (lane & 31) * 8), RLX_AGENT);
      m = asf((u32)v); l = asf((u32)(v >> 32));
      if (!__any((u32)v == SENT || (u32)(v >> 32) == SENT)) break;
    }
    const float M = wave_max(m);
    const float e = __builtin_amdgcn_exp2f((m - M) * 1.4426950408889634f);
    const float L = wave_sum(e * l) * 0.5f;
    if ((lane >> 2) == (chunk & 7)) lds[L_FW + (lane & 3)] = asu(e / L);
    TS(15);
    bar();
  }
}

extern "C" void kernel_launch(void* const* d_in, const int* in_sizes, int n_in, void* d_out, int out_size, void* d_ws, size_t ws_size,
                              hipStream_t stream) {
  fused_decode<<<NWG, NTHR, 0, stream>>>((const u16*)d_in[0], (const u16*)d_in[1], (const u16*)d_in[2], (const u16*)d_in[3],
                                         (const u16*)d_in[4], (const u16*)d_in[6], (const u16*)d_in[7], (const u16*)d_in[8],
                                         (u16*)d_out, (unsigned char*)d_ws);
}
```

```cpp
#define AB_WARM 0
#include <hip/hip_runtime.h>
#include <stdint.h>

typedef unsigned short u16;
typedef unsigned int u32;
typedef unsigned long long u64;
typedef u32 u32x2 __attribute__((ext_vector_type(2)));
typedef u32 u32x3 __attribute__((ext_vector_type(3)));
typedef u32 u32x4 __attribute__((ext_vector_type(4)));
typedef float f32x2 __attribute__((ext_vector_type(2)));
typedef float f32x4 __attribute__((ext_vector_type(4)));
typedef __bf16 bf16x2 __attribute__((ext_vector_type(2)));
#define GAS __attribute__((address_space(1)))
#define LAS __attribute__((address_space(3)))
#define RLX_AGENT __ATOMIC_RELAXED, __HIP_MEMORY_SCOPE_AGENT

#define STAMP 0

#define PH_T0 450
#define PH_DT 300
constexpr int D = 576, NH = 32, SK = 16384, DH = 128, DF = 4608;
constexpr int NWG = 256, NTHR = 1024;
constexpr u32 SENT = 0xFFFFFFFFu;
constexpr u32 SPIN_CAP = 1u << 18;
constexpr u32 OFF_EPOCH = 0, OFF_FLAGS = 4096, OFF_ML = 8192, OFF_H = 16384, OFF_Y1 = 32768, YBYTES = NWG * D * 4,
              OFF_Y3 = OFF_Y1 + YBYTES, OFF_TS = OFF_Y3 + YBYTES;
constexpr int L_QPART = 0, L_QX = L_QPART + 9 * 128, L_SC = L_QX + 4 * 64, L_PART = L_SC + 4 * 8 * 64, L_FW = L_PART + 4 * 128,
              L_ML = L_FW + 4, L_HN = L_ML + 8, L_ACT = L_HN + 5 * 64, L_GU = L_ACT + 12, L_END = L_GU + 2 * 5184;

__device__ __forceinline__ float asf(u32 v) { return __builtin_bit_cast(float, v); }
__device__ __forceinline__ u32 asu(float v) { return __builtin_bit_cast(u32, v); }
__device__ __forceinline__ float dot2(u32 a, u32 b, float c) {
  return __builtin_amdgcn_fdot2_f32_bf16(__builtin_bit_cast(bf16x2, a), __builtin_bit_cast(bf16x2, b), c, false);
}
__device__ __forceinline__ u32 pk2(float lo, float hi) {
  bf16x2 r = {(__bf16)lo, (__bf16)hi};
  return __builtin_bit_cast(u32, r);
}
__device__ __forceinline__ float rbf(float f) { return asf(pk2(f, f) << 16); }
template <int CTRL> __device__ __forceinline__ float dpp(float x) {
  return asf((u32)__builtin_amdgcn_mov_dpp((int)asu(x), CTRL, 0xf, 0xf, true));
}
template <int CTRL> __device__ __forceinline__ u32 dppu(u32 x) { return (u32)__builtin_amdgcn_mov_dpp((int)x, CTRL, 0xf, 0xf, true); }
constexpr int XOR1 = 0xB1, XOR2 = 0x4E, HMIR = 0x141, ROR8 = 0x128, ROR4 = 0x124, ROR2 = 0x122, ROR1 = 0x121, BCAST = 0x150;
__device__ __forceinline__ float sw32add(float a, float b) {
  auto s = __builtin_amdgcn_permlane32_swap(asu(a), asu(b), false, false);
  return asf(s[0]) + asf(s[1]);
}
__device__ __forceinline__ float sw16add(float a, float b) {
  auto s = __builtin_amdgcn_permlane16_swap(asu(a), asu(b), false, false);
  return asf(s[0]) + asf(s[1]);
}
__device__ __forceinline__ float row_sum(float v) {
  v += dpp<ROR8>(v); v += dpp<ROR4>(v); v += dpp<ROR2>(v); v += dpp<ROR1>(v);
  return v;
}
__device__ __forceinline__ float wave_sum(float v) { v = row_sum(v); v = sw16add(v, v); return sw32add(v, v); }
__device__ __forceinline__ float wave_max(float v) {
  v = fmaxf(v, dpp<ROR8>(v)); v = fmaxf(v, dpp<ROR4>(v)); v = fmaxf(v, dpp<ROR2>(v)); v = fmaxf(v, dpp<ROR1>(v));
  auto s = __builtin_amdgcn_permlane16_swap(asu(v), asu(v), false, false);
  v = fmaxf(asf(s[0]), asf(s[1]));
  auto t = __builtin_amdgcn_permlane32_swap(asu(v), asu(v), false, false);
  return fmaxf(asf(t[0]), asf(t[1]));
}
__device__ __forceinline__ u32 canon(float v) { const u32 u = asu(v); return u == SENT ? 0x7FC00000u : u; }
__device__ __forceinline__ void st_sc1(GAS unsigned char* p, u32 v) { __hip_atomic_store((GAS u32*)p, v, RLX_AGENT); }
__device__ __forceinline__ u32 ld_sc1(GAS unsigned char* p) { return __hip_atomic_load((GAS u32*)p, RLX_AGENT); }
__device__ __forceinline__ void bar() {
  asm volatile("s_waitcnt lgkmcnt(0)" ::: "memory");
  __builtin_amdgcn_s_barrier();
  asm volatile("" ::: "memory");
}
#define ORDER() do { asm volatile("" ::: "memory"); __builtin_amdgcn_sched_barrier(0); } while (0)
__device__ __forceinline__ void ln_stats(const float (&v)[9], float& mb, float& rden) {
  float s = ((v[0] + v[1]) + (v[2] + v[3])) + ((v[4] + v[5]) + (v[6] + v[7])) + v[8];
  const float mean = wave_sum(s) * (1.0f / D);
  float q = 0.f;
#pragma unroll
  for (int i = 0; i < 9; ++i) { const float d = v[i] - mean; q = fmaf(d, d, q); }
  const float var = wave_sum(q) * (1.0f / D);
  mb = rbf(mean);
  const float den = rbf(__builtin_amdgcn_sqrtf(rbf(rbf(var) + 1e-5f)));
  rden = __builtin_amdgcn_rcpf(den);
}
#define TS(k) do {} while (0)

__device__ __forceinline__ float gather_sum(GAS unsigned char* base, int col0, int lane) {
  const __amdgpu_buffer_rsrc_t rs = __builtin_amdgcn_make_buffer_rsrc((void*)base, 0, YBYTES, 0x00020000);
  u32 off[4];
  u32x4 v[4];
#pragma unroll
  for (int k = 0; k < 4; ++k) { off[k] = (u32)(lane + 64 * k) * (D * 4) + col0 * 4; v[k] = u32x4{0u, 0u, 0u, 0u}; }
#pragma unroll 1
  for (u32 spin = 0; spin < SPIN_CAP; ++spin) {
    u32x4 n[4];
#pragma unroll
    for (int k = 0; k < 4; ++k) n[k] = __builtin_amdgcn_raw_buffer_load_b128(rs, off[k], 0, 16);
    __builtin_amdgcn_sched_barrier(0);
    bool pend = false;
#pragma unroll
    for (int k = 0; k < 4; ++k) {
      const bool live = off[k] != 0x80000000u;
      const bool bad = n[k][0] == SENT || n[k][1] == SENT || n[k][2] == SENT || n[k][3] == SENT;
      if (live) v[k] = n[k];
      if (!bad) off[k] = 0x80000000u;
      pend |= bad;
    }
    if (!__any(pend)) break;
    asm volatile("s_sleep 1" ::: "memory");
  }
  float s[4];
#pragma unroll
  for (int e = 0; e < 4; ++e) s[e] = (asf(v[0][e]) + asf(v[1][e])) + (asf(v[2][e]) + asf(v[3][e]));
  const float z0 = sw32add(s[0], s[2]), z1 = sw32add(s[1], s[3]);
  return row_sum(sw16add(z0, z1));
}

__global__ void __launch_bounds__(NTHR) fused_decode(const u16* x, const u16* Kc, const u16* Vc,
                                                     const u16* wq, const u16* wo, const u16* wg,
                                                     const u16* wu, const u16* wd, u16* out,
                                                     unsigned char* ws_) {
  __shared__ __attribute__((aligned(16))) u32 lds[L_END];
  GAS unsigned char* ws = (GAS unsigned char*)ws_;
  const int lane = threadIdx.x & 63, wave = __builtin_amdgcn_readfirstlane(threadIdx.x >> 6);
  const int r = lane >> 4, c = lane & 15;
  const int b0 = blockIdx.x, xg = b0 & 7, idx = b0 >> 3, chunk = xg * 32 + idx, head = chunk >> 3;

  if (wave < 4) {
    const char* kb = (const char*)Kc + (size_t)chunk * 524288 + wave * 4096;
    const char* vb = (const char*)Vc + (size_t)chunk * 524288 + wave * 4096;
    const u32 lo = lane * 16;
    const int rot = b0 * 37;
    u32x4 buf[16];
#define ISSUE4(base, n, bq) do { const char* p_ = (base) + (size_t)((((n) + rot) & 31) * 16384); \
    _Pragma("unroll") for (int t_ = 0; t_ < 4; ++t_) buf[(bq) * 4 + t_] = __builtin_nontemporal_load((const u32x4*)(p_ + lo + t_ * 1024)); } while (0)
    TS(0);
    bar();
#pragma unroll
    for (int bq = 0; bq < 4; ++bq) ISSUE4(kb, bq, bq);
    bar();
    u32x4 q;
    {
      f32x2 a = {0.f, 0.f};
#pragma unroll
      for (int j = 0; j < 9; ++j) a += *(const f32x2*)&lds[L_QPART + j * 128 + 2 * lane];
      lds[L_QX + wave * 64 + lane] = pk2(a[0], a[1]);
      asm volatile("s_waitcnt lgkmcnt(0)" ::: "memory");
      q = *(const u32x4*)&lds[L_QX + wave * 64 + 4 * c];
    }
    if (wave == 0) TS(1);
    u32* sc = &lds[L_SC + wave * 512 + lane];
#pragma unroll 1
    for (int g = 0; g < 8; ++g) {
      const char* nb = g < 7 ? kb : vb;
      const int nn = (4 * g + 4) & 31;
      float sreg = 0.f;
#pragma unroll
      for (int bq = 0; bq < 4; ++bq) {
        float xs[4];
#pragma unroll
        for (int t = 0; t < 4; ++t) {
          const u32x4 k = buf[bq * 4 + t];
          xs[t] = dot2(k[3], q[3], dot2(k[2], q[2], dot2(k[1], q[1], dot2(k[0], q[0], 0.f))));
        }
        asm volatile("" : "+v"(xs[0]), "+v"(xs[1]), "+v"(xs[2]), "+v"(xs[3]));
        ISSUE4(nb, nn + bq, bq);
        __builtin_amdgcn_sched_barrier(0);
        const float a0 = xs[0] + dpp<ROR8>(xs[0]), a1 = xs[1] + dpp<ROR8>(xs[1]), a2 = xs[2] + dpp<ROR8>(xs[2]), a3 = xs[3] + dpp<ROR8>(xs[3]);
        const float y0 = (c & 8) ? a2 : a0, y1 = (c & 8) ? a3 : a1;
        const float e0 = y0 + dpp<HMIR>(y0), e1 = y1 + dpp<HMIR>(y1);
        float z = (c & 4) ? e1 : e0;
        z += dpp<XOR2>(z); z += dpp<XOR1>(z);
        sreg = ((c & 3) == bq) ? z : sreg;
      }
      sc[g * 64] = asu(rbf(rbf(sreg) * 0.08838834764831845f));
    }
    if (wave == 0) TS(2);
    {
      float s[8];
#pragma unroll
      for (int g = 0; g < 8; ++g) s[g] = asf(sc[g * 64]);
      const float m = wave_max(fmaxf(fmaxf(fmaxf(s[0], s[1]), fmaxf(s[2], s[3])), fmaxf(fmaxf(s[4], s[5]), fmaxf(s[6], s[7]))));
      float l = 0.f;
#pragma unroll
      for (int g = 0; g < 8; ++g) { const float p = __builtin_amdgcn_exp2f((s[g] - m) * 1.4426950408889634f); l += p; sc[g * 64] = asu(p); }
      l = wave_sum(l);
      if (lane == 0) *(volatile LAS u64*)&lds[L_ML + 2 * wave] = ((u64)canon(l) << 32) | canon(m);
    }
    float acc[8];
#pragma unroll
    for (int k = 0; k < 8; ++k) acc[k] = 0.f;
#define PV4(bq, pg) do { \
    _Pragma("unroll") for (int t_ = 0; t_ < 4; ++t_) { \
      const float pb_ = t_ == 0 ? dpp<BCAST + (bq)>(pg) : t_ == 1 ? dpp<BCAST + 4 + (bq)>(pg) : t_ == 2 ? dpp<BCAST + 8 + (bq)>(pg) : dpp<BCAST + 12 + (bq)>(pg); \
      const u32x4 v_ = buf[(bq) * 4 + t_]; \
      _Pragma("unroll") for (int e_ = 0; e_ < 4; ++e_) { \
        acc[2 * e_] = fmaf(pb_, asf(v_[e_] << 16), acc[2 * e_]); acc[2 * e_ + 1] = fmaf(pb_, asf(v_[e_] & 0xffff0000u), acc[2 * e_ + 1]); } } \
    asm volatile("" : "+v"(acc[0]), "+v"(acc[1]), "+v"(acc[2]), "+v"(acc[3]), "+v"(acc[4]), "+v"(acc[5]), "+v"(acc[6]), "+v"(acc[7])); } while (0)
#pragma unroll 1
    for (int g = 0; g < 7; ++g) {
      const float pg = asf(sc[g * 64]);
      const int nn = 4 * g + 4;
      PV4(0, pg); ISSUE4(vb, nn + 0, 0); __builtin_amdgcn_sched_barrier(0);
      PV4(1, pg); ISSUE4(vb, nn + 1, 1); __builtin_amdgcn_sched_barrier(0);
      PV4(2, pg); ISSUE4(vb, nn + 2, 2); __builtin_amdgcn_sched_barrier(0);
      PV4(3, pg); ISSUE4(vb, nn + 3, 3); __builtin_amdgcn_sched_barrier(0);
    }
    {
      const float pg = asf(sc[7 * 64]);
      PV4(0, pg); PV4(1, pg); PV4(2, pg); PV4(3, pg);
    }
    const float b0_ = sw32add(acc[0], acc[4]), b1_ = sw32add(acc[1], acc[5]), b2_ = sw32add(acc[2], acc[6]), b3_ = sw32add(acc[3], acc[7]);
    const f32x2 e = {sw16add(b0_, b2_), sw16add(b1_, b3_)};
    *(f32x2*)&lds[L_PART + wave * 128 + 8 * c + 2 * r] = e;
    TS(4 + wave);
    bar();
    return;
  }

  if (wave < 13) {
    const int j = wave - 4;
    __builtin_amdgcn_s_setprio(2);
    const u64 tstart = __builtin_amdgcn_s_memrealtime();
    u16 xr[9];
#pragma unroll
    for (int i = 0; i < 9; ++i) xr[i] = x[lane + 64 * i];
    const u16 xo = x[64 * j + lane];
    ORDER();
    u32x4 wqv[16];
    {
      const __amdgpu_buffer_rsrc_t rq = __builtin_amdgcn_make_buffer_rsrc((void*)wq, 0, D * NH * DH * 2, 0x00020000);
      const u32 vo = r * (16 * 8192) + c * 16;
      const u32 so = (64 * j) * 8192 + head * 256;
#pragma unroll
      for (int i = 0; i < 16; ++i) {
        wqv[i] = __builtin_amdgcn_raw_buffer_load_b128(rq, vo, so + i * 8192, 2);
        ORDER();
      }
    }
    bar();
    u16 wdr[18];
    {
      const u16* p = wd + (size_t)(18 * chunk) * D + 64 * j + lane;
#pragma unroll
      for (int k = 0; k < 18; ++k)
        wdr[k] = __builtin_nontemporal_load(p + k * D);
    }
    ORDER();
    {
      float xv[9];
#pragma unroll
      for (int i = 0; i < 9; ++i) xv[i] = asf((u32)xr[i] << 16);
      const float xown = asf((u32)xo << 16);
      float mb, rden;
      ln_stats(xv, mb, rden);
      const u32 xh = asu(rbf(rbf(xown - mb) * rden));
      const u32 xl = xh >> 16;
      float qa[8];
#pragma unroll
      for (int k = 0; k < 8; ++k) qa[k] = 0.f;
#define QROW(i) do { const u32 l_ = dppu<BCAST + (i)>(xl), h_ = dppu<BCAST + (i)>(xh); \
      _Pragma("unroll") for (int e_ = 0; e_ < 4; ++e_) { qa[2 * e_] = dot2(wqv[i][e_], l_, qa[2 * e_]); qa[2 * e_ + 1] = dot2(wqv[i][e_], h_, qa[2 * e_ + 1]); } } while (0)
      QROW(0); QROW(1); QROW(2); QROW(3); QROW(4); QROW(5); QROW(6); QROW(7);
      QROW(8); QROW(9); QROW(10); QROW(11); QROW(12); QROW(13); QROW(14); QROW(15);
      const float b0_ = sw32add(qa[0], qa[4]), b1_ = sw32add(qa[1], qa[5]), b2_ = sw32add(qa[2], qa[6]), b3_ = sw32add(qa[3], qa[7]);
      const f32x2 e = {sw16add(b0_, b2_), sw16add(b1_, b3_)};
      *(f32x2*)&lds[L_QPART + j * 128 + 8 * c + 2 * r] = e;
    }
    bar();
    ORDER();
    u32 wdv[9];
#pragma unroll
    for (int k = 0; k < 9; ++k) { wdv[k] = (u32)wdr[2 * k] | ((u32)wdr[2 * k + 1] << 16); asm volatile("" : "+v"(wdv[k])); }
    {
      const u64 tgo = tstart + (u64)(PH_T0 + PH_DT * j);
#pragma unroll 1
      while (__builtin_amdgcn_s_memrealtime() < tgo) __builtin_amdgcn_s_sleep(16);
    }
    ORDER();
    u32 tch[2];
    tch[0] = tch[1] = 0;
    u32x2 wov[32];
    {
      const char* p = (const char*)wo + (size_t)(head * 128) * (D * 2) + j * 128;
      const u32 vo = r * (32 * D * 2) + c * 8;
#pragma unroll
      for (int i = 0; i < 32; ++i)
        wov[i] = __builtin_nontemporal_load((const u32x2*)(p + i * (D * 2) + vo));
    }
    ORDER();
    asm volatile("" :: "v"(tch[0]), "v"(tch[1]));
    u32 W[16][4];
#pragma unroll
    for (int pi = 0; pi < 16; ++pi) {
      const u32x2 A = wov[2 * pi], B = wov[2 * pi + 1];
      W[pi][0] = __builtin_amdgcn_perm(B[0], A[0], 0x05040100); W[pi][1] = __builtin_amdgcn_perm(B[0], A[0], 0x07060302);
      W[pi][2] = __builtin_amdgcn_perm(B[1], A[1], 0x05040100); W[pi][3] = __builtin_amdgcn_perm(B[1], A[1], 0x07060302);
      asm volatile("" : "+v"(W[pi][0]), "+v"(W[pi][1]), "+v"(W[pi][2]), "+v"(W[pi][3]));
    }
    ORDER();
    {
      u32x3 gv[3], uv[3];
#pragma unroll
      for (int k = 0; k < 3; ++k) {
        const u32 p = (j * 3 + k) * 64 + lane, row = p / 3, th = p - row * 3;
        const u32 o = row * (DF * 2) + chunk * 36 + th * 12;
        gv[k] = __builtin_nontemporal_load((const u32x3*)((const char*)wg + o));
        uv[k] = __builtin_nontemporal_load((const u32x3*)((const char*)wu + o));
      }
#pragma unroll
      for (int k = 0; k < 3; ++k) {
        const int p = (j * 3 + k) * 64 + lane;
#pragma unroll
        for (int e = 0; e < 3; ++e) { lds[L_GU + p * 3 + e] = gv[k][e]; lds[L_GU + 5184 + p * 3 + e] = uv[k][e]; }
      }
    }
    if (j == 8) TS(3);
    bar();
    if (j == 0) TS(8);
    {
      const f32x4 f = *(const f32x4*)&lds[L_FW];
      f32x2 a = f[0] * *(const f32x2*)&lds[L_PART + 2 * lane];
      a += f[1] * *(const f32x2*)&lds[L_PART + 128 + 2 * lane];
      a += f[2] * *(const f32x2*)&lds[L_PART + 256 + 2 * lane];
      a += f[3] * *(const f32x2*)&lds[L_PART + 384 + 2 * lane];
      const u32 ap = pk2(a[0], a[1]);
      float y[4] = {0.f, 0.f, 0.f, 0.f};
#define WOROW(pi) do { const u32 a_ = dppu<BCAST + (pi)>(ap); \
      _Pragma("unroll") for (int e_ = 0; e_ < 4; ++e_) y[e_] = dot2(W[pi][e_], a_, y[e_]); } while (0)
      WOROW(0); WOROW(1); WOROW(2); WOROW(3); WOROW(4); WOROW(5); WOROW(6); WOROW(7);
      WOROW(8); WOROW(9); WOROW(10); WOROW(11); WOROW(12); WOROW(13); WOROW(14); WOROW(15);
      const float yy = sw16add(sw32add(y[0], y[2]), sw32add(y[1], y[3]));
      st_sc1(ws + OFF_Y1 + chunk * (D * 4) + (64 * j + 4 * c + r) * 4, canon(yy));
    }
    if (j == 0) TS(9);
    __builtin_amdgcn_sched_barrier(0);
    u32 G[5][2], U[5][2];
    {
      u32 gw[9], uw[9];
#pragma unroll
      for (int i = 0; i < 9; ++i) { gw[i] = lds[L_GU + (lane + 64 * i) * 9 + j]; uw[i] = lds[L_GU + 5184 + (lane + 64 * i) * 9 + j]; }
#pragma unroll
      for (int k = 0; k < 4; ++k) {
        G[k][0] = __builtin_amdgcn_perm(gw[2 * k + 1], gw[2 * k], 0x05040100); G[k][1] = __builtin_amdgcn_perm(gw[2 * k + 1], gw[2 * k], 0x07060302);
        U[k][0] = __builtin_amdgcn_perm(uw[2 * k + 1], uw[2 * k], 0x05040100); U[k][1] = __builtin_amdgcn_perm(uw[2 * k + 1], uw[2 * k], 0x07060302);
      }
      G[4][0] = gw[8] & 0xffffu; G[4][1] = gw[8] >> 16; U[4][0] = uw[8] & 0xffffu; U[4][1] = uw[8] >> 16;
#pragma unroll
      for (int k = 0; k < 5; ++k) asm volatile("" : "+v"(G[k][0]), "+v"(G[k][1]), "+v"(U[k][0]), "+v"(U[k][1]));
    }
    bar();
    if (j == 0) TS(12);
    {
      float g0 = 0.f, g1 = 0.f, u0 = 0.f, u1 = 0.f;
#pragma unroll
      for (int k = 0; k < 5; ++k) {
        const u32 hp = lds[L_HN + k * 64 + lane];
        g0 = dot2(G[k][0], hp, g0); g1 = dot2(G[k][1], hp, g1); u0 = dot2(U[k][0], hp, u0); u1 = dot2(U[k][1], hp, u1);
      }
      const float v = rbf(row_sum(sw16add(sw32add(g0, g1), sw32add(u0, u1))));
      auto s = __builtin_amdgcn_permlane16_swap(asu(v), asu(v), false, false);
      const float gg = asf(s[0]), uu = asf(s[1]);
      const float t = gg * fmaf(gg * gg, 0.044715f, 1.0f) * (-2.0f * 0.7978845608028654f * 1.4426950408889634f);
      const float ge = rbf(gg * __builtin_amdgcn_rcpf(1.0f + __builtin_amdgcn_exp2f(t)));
      const u32 a = pk2(ge * uu, 0.f);
      if ((lane & 31) == 0) ((u16*)&lds[L_ACT])[2 * j + (lane >> 5)] = (u16)a;
    }
    bar();
    {
      const u32x4 a0 = *(const u32x4*)&lds[L_ACT], a1 = *(const u32x4*)&lds[L_ACT + 4];
      const u32 a2 = lds[L_ACT + 8];
      float y0 = dot2(wdv[0], a0[0], 0.f), y1 = dot2(wdv[1], a0[1], 0.f);
      y0 = dot2(wdv[2], a0[2], y0); y1 = dot2(wdv[3], a0[3], y1);
      y0 = dot2(wdv[4], a1[0], y0); y1 = dot2(wdv[5], a1[1], y1);
      y0 = dot2(wdv[6], a1[2], y0); y1 = dot2(wdv[7], a1[3], y1);
      y0 = dot2(wdv[8], a2, y0);
      st_sc1(ws + OFF_Y3 + chunk * (D * 4) + (64 * j + lane) * 4, canon(y0 + y1));
    }
    if (j == 0) TS(13);
    return;
  }

  __builtin_amdgcn_s_setprio(3);
  const int line = xg + 8 * (idx >> 3), col0 = 32 * line + 4 * (idx & 7);
  if (wave == 13) {
    const bool act = line < 18;
    u64 ep = 0;
    bar();
    bar();
    const float xv = act ? asf((u32)x[col0 + r] << 16) : 0.f;
    if (b0 == 0) ep = __hip_atomic_load((GAS u64*)(ws + OFF_EPOCH), RLX_AGENT);
    bar();
    if (!act) return;
    const float hv = rbf(xv + rbf(gather_sum(ws + OFF_Y1, col0, lane)));
    if (c == 0) st_sc1(ws + OFF_H + (col0 + r) * 4, canon(hv));
    TS(10);
    bar();
    bar();
    const float o = hv + rbf(gather_sum(ws + OFF_Y3, col0, lane));
    if (c == 0) out[col0 + r] = (u16)pk2(o, 0.f);
    if (b0 == 0 && lane == 0) *(GAS u64*)(ws + OFF_EPOCH) = ep * 0x9E3779B97F4A7C15ull + 0x632BE59BD9B4E019ull;
    TS(14);
    return;
  }

  if (wave == 14) {
    bar();
    bar();
    bar();
    float hv[9];
#pragma unroll 1
    for (u32 spin = 0; spin < SPIN_CAP; ++spin) {
      bool bad = false;
#pragma unroll
      for (int i = 0; i < 9; ++i) { const u32 u = ld_sc1(ws + OFF_H + (lane + 64 * i) * 4); hv[i] = asf(u); bad |= u == SENT; }
      if (!__any(bad)) break;
      asm volatile("s_sleep 1" ::: "memory");
    }
    TS(11);
    float mb, rden;
    ln_stats(hv, mb, rden);
#pragma unroll
    for (int i = 0; i < 9; ++i) hv[i] = rbf(hv[i] - mb) * rden;
#pragma unroll
    for (int k = 0; k < 4; ++k) lds[L_HN + k * 64 + lane] = pk2(hv[2 * k], hv[2 * k + 1]);
    lds[L_HN + 256 + lane] = pk2(hv[8], 0.f);
    bar();
    return;
  }

  {
    if (lane < 8) lds[L_ML + lane] = SENT;
    bar();
    bar();
    const u64 ep = __hip_atomic_load((GAS u64*)(ws + OFF_EPOCH), RLX_AGENT);
    {
      const __amdgpu_buffer_rsrc_t r1 = __builtin_amdgcn_make_buffer_rsrc((void*)(ws + OFF_Y1 + chunk * (D * 4)), 0, D * 4, 0x00020000);
      const __amdgpu_buffer_rsrc_t r3 = __builtin_amdgcn_make_buffer_rsrc((void*)(ws + OFF_Y3 + chunk * (D * 4)), 0, D * 4, 0x00020000);
      const u32x4 s4 = {SENT, SENT, SENT, SENT};
#pragma unroll
      for (int k = 0; k < 3; ++k) {
        __builtin_amdgcn_raw_buffer_store_b128(s4, r1, k * 1024 + lane * 16, 0, 16);
        __builtin_amdgcn_raw_buffer_store_b128(s4, r3, k * 1024 + lane * 16, 0, 16);
      }
      if (lane < 8) st_sc1(ws + OFF_ML + chunk * 32 + lane * 4, SENT);
      if (lane < 4 && line < 18) st_sc1(ws + OFF_H + (col0 + lane) * 4, SENT);
    }
    asm volatile("s_waitcnt vmcnt(0)" ::: "memory");
    const u64 tag = ep * 0x9E3779B97F4A7C15ull + 0x632BE59BD9B4E019ull;
    if (lane == 0) __hip_atomic_store((GAS u64*)(ws + OFF_FLAGS + b0 * 8), tag, RLX_AGENT);
#pragma unroll 1
    for (u32 spin = 0; spin < SPIN_CAP; ++spin) {
      asm volatile("s_sleep 64" ::: "memory");
      bool bad = false;
#pragma unroll
      for (int k = 0; k < 4; ++k) bad |= __hip_atomic_load((GAS u64*)(ws + OFF_FLAGS + (lane + 64 * k) * 8), RLX_AGENT) != tag;
      if (!__any(bad)) break;
    }
#pragma unroll 1
    for (u32 spin = 0; spin < SPIN_CAP; ++spin) {
      asm volatile("s_sleep 32" ::: "memory");
      const u64 v = *(volatile LAS u64*)&lds[L_ML + 2 * (lane & 3)];
      if (!__any((u32)v == SENT || (u32)(v >> 32) == SENT)) {
        if (lane < 4) __hip_atomic_store((GAS u64*)(ws + OFF_ML + (chunk * 4 + lane) * 8), v, RLX_AGENT);
        break;
      }
    }
    float m, l;
#pragma unroll 1
    for (u32 spin = 0; spin < SPIN_CAP; ++spin) {
      asm volatile("s_sleep 64" ::: "memory");
      const u64 v = __hip_atomic_load((GAS u64*)(ws + OFF_ML + head * 256 + (lane & 31) * 8), RLX_AGENT);
      m = asf((u32)v); l = asf((u32)(v >> 32));
      if (!__any((u32)v == SENT || (u32)(v >> 32) == SENT)) break;
    }
    const float M = wave_max(m);
    const float e = __builtin_amdgcn_exp2f((m - M) * 1.4426950408889634f);
    const float L = wave_sum(e * l) * 0.5f;
    if ((lane >> 2) == (chunk & 7)) lds[L_FW + (lane & 3)] = asu(e / L);
    TS(15);
    bar();
  }
}

extern "C" void kernel_launch(void* const* d_in, const int* in_sizes, int n_in, void* d_out, int out_size, void* d_ws, size_t ws_size,
                              hipStream_t stream) {
  fused_decode<<<NWG, NTHR, 0, stream>>>((const u16*)d_in[0], (const u16*)d_in[1], (const u16*)d_in[2], (const u16*)d_in[3],
                                         (const u16*)d_in[4], (const u16*)d_in[6], (const u16*)d_in[7], (const u16*)d_in[8],
                                         (u16*)d_out, (unsigned char*)d_ws);
}
```

```cpp
#include <hip/hip_runtime.h>
#include <stdint.h>

typedef unsigned short u16;
typedef unsigned int u32;
typedef unsigned long long u64;
typedef u32 u32x2 __attribute__((ext_vector_type(2)));
typedef u32 u32x4 __attribute__((ext_vector_type(4)));
typedef float f32x2 __attribute__((ext_vector_type(2)));
typedef float f32x4 __attribute__((ext_vector_type(4)));
typedef __bf16 bf16x2 __attribute__((ext_vector_type(2)));
#define GAS __attribute__((address_space(1)))
#define LAS __attribute__((address_space(3)))
#define RLX_AGENT __ATOMIC_RELAXED, __HIP_MEMORY_SCOPE_AGENT

#define STAMP 0
#define PH_T0 500
#define PH_DT 36
#define AUX_W 2
#define PACE_ON 1
#define PC_N 6
#define PC_T0 800
#define PC_DT 400
#define PC_LAG 200
#define PC_EPS 0.01f
#define PC_MARG 0.005f
#define PC_LEAD 0.2f
#define DEF_M 22
#define DEF_S 34
#define QDELAY 16
#define CODE_SLICE 336
#define K_INFL 8
#define PRE 2
#define DELAY1 0
#define DELAY3 24

constexpr int D = 576, NH = 32, DH = 128, DF = 4608;
constexpr int NWG = 256, NTHR = 1024, NMLP = 72;
constexpr u32 SENT = 0xFFFFFFFFu, OOB = 0x80000000u;
constexpr u32 SPIN_CAP = 1u << 18;
constexpr float LOG2E = 1.4426950408889634f;
constexpr u32 ROWB = D * 4;
constexpr u32 OFF_EPOCH = 0, OFF_FLAGS = 4096, OFF_ML = 8192, OFF_PR = 16384, OFF_Q = OFF_PR + 8 * 4096, OFF_P = OFF_Q + NWG * 1024, PREP_BYTES = 8 * ROWB,
              OFF_Y1 = OFF_P + 8 * PREP_BYTES, OFF_Y3 = OFF_Y1 + NWG * ROWB, OFF_TS = OFF_Y3 + NMLP * ROWB;
constexpr int MAXBLK = 144;
constexpr u32 TK0 = 16 + 9 * PRE, NKW = PRE ? 13 : 4;
constexpr int L_QPART = 0, L_Q = L_QPART + 9 * 64, L_XN = L_Q + 64, L_SC = L_XN + 288, L_PART = L_SC + MAXBLK * 16, L_CTL = L_PART + 4 * 128,
              L_ST = L_CTL + 16, L_GU = L_ST + 32, L_WD = L_GU + 9 * 128, L_END = L_WD + 9 * 8 * 256;
constexpr int C_MX = L_CTL + 3, C_TK = L_CTL + 4, C_KDONE = L_CTL + 5, C_QCNT = L_CTL + 6, C_QRDY = L_CTL + 7, C_XRDY = L_CTL + 8, C_F = L_CTL + 9, C_PACE = L_CTL + 10;

__device__ __forceinline__ float asf(u32 v) { return __builtin_bit_cast(float, v); }
__device__ __forceinline__ u32 asu(float v) { return __builtin_bit_cast(u32, v); }
__device__ __forceinline__ float dot2(u32 a, u32 b, float c) {
  return __builtin_amdgcn_fdot2_f32_bf16(__builtin_bit_cast(bf16x2, a), __builtin_bit_cast(bf16x2, b), c, false);
}
__device__ __forceinline__ u32 pk2(float lo, float hi) {
  bf16x2 r = {(__bf16)lo, (__bf16)hi};
  return __builtin_bit_cast(u32, r);
}
__device__ __forceinline__ float rbf(float f) { return asf(pk2(f, f) << 16); }
__device__ __forceinline__ u32 lo2(u32 a, u32 b) { return __builtin_amdgcn_perm(b, a, 0x05040100); }
__device__ __forceinline__ u32 hi2(u32 a, u32 b) { return __builtin_amdgcn_perm(b, a, 0x07060302); }
template <int CTRL> __device__ __forceinline__ float dpp(float x) {
  return asf((u32)__builtin_amdgcn_mov_dpp((int)asu(x), CTRL, 0xf, 0xf, true));
}
template <int CTRL> __device__ __forceinline__ u32 dppu(u32 x) { return (u32)__builtin_amdgcn_mov_dpp((int)x, CTRL, 0xf, 0xf, true); }
constexpr int XOR1 = 0xB1, XOR2 = 0x4E, HMIR = 0x141, ROR8 = 0x128, ROR4 = 0x124, ROR2 = 0x122, ROR1 = 0x121, BCAST = 0x150;
__device__ __forceinline__ float sw32add(float a, float b) {
  auto s = __builtin_amdgcn_permlane32_swap(asu(a), asu(b), false, false);
  return asf(s[0]) + asf(s[1]);
}
__device__ __forceinline__ float sw16add(float a, float b) {
  auto s = __builtin_amdgcn_permlane16_swap(asu(a), asu(b), false, false);
  return asf(s[0]) + asf(s[1]);
}
__device__ __forceinline__ float row_sum(float v) {
  v += dpp<ROR8>(v); v += dpp<ROR4>(v); v += dpp<ROR2>(v); v += dpp<ROR1>(v);
  return v;
}
__device__ __forceinline__ float wave_sum(float v) { v = row_sum(v); v = sw16add(v, v); return sw32add(v, v); }
__device__ __forceinline__ float wave_max(float v) {
  v = fmaxf(v, dpp<ROR8>(v)); v = fmaxf(v, dpp<ROR4>(v)); v = fmaxf(v, dpp<ROR2>(v)); v = fmaxf(v, dpp<ROR1>(v));
  auto s = __builtin_amdgcn_permlane16_swap(asu(v), asu(v), false, false);
  v = fmaxf(asf(s[0]), asf(s[1]));
  auto t = __builtin_amdgcn_permlane32_swap(asu(v), asu(v), false, false);
  return fmaxf(asf(t[0]), asf(t[1]));
}
__device__ __forceinline__ u64 next_tag(u64 ep) { return ep * 0x9E3779B97F4A7C15ull + 0x632BE59BD9B4E019ull; }
__device__ __forceinline__ u32 canon(float v) { return min(asu(v), SENT - 1u); }
__device__ __forceinline__ void st_sc1(GAS unsigned char* p, u32 v) { __hip_atomic_store((GAS u32*)p, v, RLX_AGENT); }
__device__ __forceinline__ u32 ld_sc1(GAS unsigned char* p) { return __hip_atomic_load((GAS u32*)p, RLX_AGENT); }
__device__ __forceinline__ void bar() {
  asm volatile("s_waitcnt lgkmcnt(0)" ::: "memory");
  __builtin_amdgcn_s_barrier();
  asm volatile("" ::: "memory");
}
template <typename T> __device__ __forceinline__ __amdgpu_buffer_rsrc_t rsrc(T p, u32 bytes) {
  return __builtin_amdgcn_make_buffer_rsrc((void*)(u64)p, 0, bytes, 0x00020000);
}
#define ORDER() do { asm volatile("" ::: "memory"); __builtin_amdgcn_sched_barrier(0); } while (0)
__device__ __forceinline__ void ln_finish(float mean, float var, float& mb, float& rden) {
  mb = rbf(mean);
  const float den = rbf(__builtin_amdgcn_sqrtf(rbf(rbf(var) + 1e-5f)));
  rden = __builtin_amdgcn_rcpf(den);
}
#define TS(k) do {} while (0)

template <int N> __device__ __forceinline__ void gather(__amdgpu_buffer_rsrc_t rs, u32 (&off)[N], u32x4 (&v)[N]) {
#pragma unroll
  for (int k = 0; k < N; ++k) v[k] = u32x4{0u, 0u, 0u, 0u};
#pragma unroll 1
  for (u32 spin = 0; spin < SPIN_CAP; ++spin) {
    u32x4 n[N];
#pragma unroll
    for (int k = 0; k < N; ++k) n[k] = __builtin_amdgcn_raw_buffer_load_b128(rs, off[k], 0, 16);
    __builtin_amdgcn_sched_barrier(0);
    bool pend = false;
#pragma unroll
    for (int k = 0; k < N; ++k) {
      const bool bad = max(max(n[k][0], n[k][1]), max(n[k][2], n[k][3])) == SENT;
      if (off[k] != OOB) v[k] = n[k];
      if (!bad) off[k] = OOB;
      pend |= bad;
    }
    if (!__any(pend)) break;
    asm volatile("s_sleep 1" ::: "memory");
  }
}
__device__ __forceinline__ float reduce4(const u32x4 a, const u32x4 b) {
  return row_sum(sw16add(sw32add(asf(a[0]) + asf(b[0]), asf(a[2]) + asf(b[2])), sw32add(asf(a[1]) + asf(b[1]), asf(a[3]) + asf(b[3]))));
}
__device__ __forceinline__ float reduce8(const float (&a)[8], int c) {
  const float b0 = sw32add(a[0], a[4]), b1 = sw32add(a[1], a[5]), b2 = sw32add(a[2], a[6]), b3 = sw32add(a[3], a[7]);
  const float e0 = sw16add(b0, b2), e1 = sw16add(b1, b3);
  const float f0 = e0 + dpp<ROR8>(e0), f1 = e1 + dpp<ROR8>(e1);
  return (c & 8) ? f1 : f0;
}
__device__ __forceinline__ u32 pair_up(float v) { return pk2(v, dpp<XOR1>(v)); }
typedef __bf16 bf16x8 __attribute__((ext_vector_type(8)));
template <int S, int ST> __device__ __forceinline__ bf16x8 afrag(u32 ap) {
  const u32x4 a = {dppu<BCAST + ST * (4 * S)>(ap), dppu<BCAST + ST * (4 * S + 1)>(ap), dppu<BCAST + ST * (4 * S + 2)>(ap), dppu<BCAST + ST * (4 * S + 3)>(ap)};
  return __builtin_bit_cast(bf16x8, a);
}
__device__ __forceinline__ void mstep(const bf16x8 a, const u32x4 (&B)[4], f32x4 (&d)[4]) {
#pragma unroll
  for (int e = 0; e < 4; ++e) d[e] = __builtin_amdgcn_mfma_f32_16x16x32_bf16(a, __builtin_bit_cast(bf16x8, B[e]), d[e], 0, 0, 0);
}
__device__ __forceinline__ u32x4 mout(const f32x4 (&d)[4]) { return u32x4{canon(d[0][0]), canon(d[1][0]), canon(d[2][0]), canon(d[3][0])}; }
__device__ __forceinline__ void put_pair(u32x4 (&B)[4], int p, const u32x2 lo, const u32x2 hi) {
  B[0][p & 3] = lo2(lo[0], hi[0]); B[1][p & 3] = hi2(lo[0], hi[0]); B[2][p & 3] = lo2(lo[1], hi[1]); B[3][p & 3] = hi2(lo[1], hi[1]);
}
#define PIN4(B) asm volatile("" : "+v"((B)[0]), "+v"((B)[1]), "+v"((B)[2]), "+v"((B)[3]))

__global__ void __launch_bounds__(NTHR) fused_decode(const u16* x, const u16* Kc, const u16* wq, unsigned char* ws_, const u16* Vc, const u16* wo,
                                                     const u16* wg, const u16* wu, const u16* wd, u16* out) {
  __shared__ __attribute__((aligned(16))) u32 lds[L_END];
  u64 pc0;
  asm volatile("s_getpc_b64 %0" : "=s"(pc0));
  pc0 &= ~255ull;
  GAS unsigned char* ws = (GAS unsigned char*)ws_;
  const int lane = threadIdx.x & 63, wave = __builtin_amdgcn_readfirstlane(threadIdx.x >> 6);
  const int r = lane >> 4, c = lane & 15;
  const int b0 = blockIdx.x, xg = b0 & 7, idx = b0 >> 3, chunk = xg * 32 + idx, head = chunk >> 3, hh = idx >> 3, sp = idx & 7;
#define SPLIT() \
  int sp_ = sp; asm volatile("" : "+s"(sp_)); \
  const int d7 = (hh == 0 || (hh == 1 && xg == 0)) ? DEF_S : DEF_M, d5 = hh == 2 ? DEF_M : 0, dsum = 1024 + d5 + DEF_M + d7; \
  const int blk0 = ((sp_ * dsum) >> 3) - (sp_ > 5 ? d5 : 0) - (sp_ > 6 ? DEF_M : 0); \
  const int nblk = (((sp_ + 1) * dsum) >> 3) - (sp_ >= 5 ? d5 : 0) - (sp_ >= 6 ? DEF_M : 0) - (sp_ == 7 ? d7 : 0) - blk0; \
  const int rot0 = (32 * (sp_ & 3) - blk0) & 127, rot = rot0 + 16 <= nblk ? rot0 : rot0 - 16 <= nblk ? rot0 - 32 : rot0 - 64; \
  const u32 kvoff = (u32)(head * 1024 + blk0) * 4096u; \
  (void)rot; (void)kvoff
  const bool mlp = sp >= 6 || (sp == 5 && hh == 2);
  const u32 lo = lane * 16;

#define KSCORE(B, cur, q, mx) do { \
    float xs[4]; \
    _Pragma("unroll") for (int t = 0; t < 4; ++t) { \
      const u32x4 k = (B)[t]; \
      xs[t] = dot2(k[3], q[3], dot2(k[2], q[2], dot2(k[1], q[1], dot2(k[0], q[0], 0.f)))); } \
    const float a0 = xs[0] + dpp<ROR8>(xs[0]), a1 = xs[1] + dpp<ROR8>(xs[1]), a2 = xs[2] + dpp<ROR8>(xs[2]), a3 = xs[3] + dpp<ROR8>(xs[3]); \
    const float y0 = (c & 8) ? a2 : a0, y1 = (c & 8) ? a3 : a1; \
    const float e0 = y0 + dpp<HMIR>(y0), e1 = y1 + dpp<HMIR>(y1); \
    float z = (c & 4) ? e1 : e0; \
    z += dpp<XOR2>(z); z += dpp<XOR1>(z); \
    z = rbf(rbf(z) * 0.08838834764831845f); \
    mx = fmaxf(mx, z); \
    lds[L_SC + (cur) * 16 + 4 * r + (c >> 2)] = asu(z);     \
  } while (0)
#define KDONE(mx) do { mx = wave_max(mx); \
    if (lane == 0) { __hip_atomic_fetch_max((LAS float*)&lds[C_MX], mx, __ATOMIC_RELAXED, __HIP_MEMORY_SCOPE_WORKGROUP); \
                     asm volatile("s_waitcnt lgkmcnt(0)" ::: "memory"); \
                     __hip_atomic_fetch_add((LAS u32*)&lds[C_KDONE], 1u, __ATOMIC_RELAXED, __HIP_MEMORY_SCOPE_WORKGROUP); } } while (0)
#define WAIT_Q() do { _Pragma("unroll 1") for (u32 spin = 0; spin < SPIN_CAP * 16; ++spin) { \
      if (*(volatile LAS u32*)&lds[C_QRDY] != 0u) break; \
      __builtin_amdgcn_s_sleep(1); } \
    asm volatile("" ::: "memory"); } while (0)
#define ARGS_READY() bar()

  if (wave < 4) {
    SPLIT();
    u32x4 buf[16];
    u32 off0 = kvoff + (rot + wave) * 4096;
    asm volatile("" : "+s"(off0));
    ARGS_READY();
    const u32 tstart = (u32)__builtin_amdgcn_s_memrealtime();
    {
      const __amdgpu_buffer_rsrc_t rs = rsrc((u64)Kc + off0, 65536u);
#pragma unroll
      for (int u = 0; u < 16; ++u) {
        if (u >= K_INFL) asm volatile("s_waitcnt vmcnt(%0)" :: "i"(K_INFL - 1) : "memory");
        buf[u] = __builtin_amdgcn_raw_buffer_load_b128(rs, lo + (u & 3) * 1024, (u >> 2) * 16384, 2);
        ORDER();
      }
    }
    const u64 kb = (u64)Kc + kvoff;
    ORDER();
    const u64 vb = (u64)Vc + kvoff;
    int id[4] = {wave, 4 + wave, 8 + wave, 12 + wave};
#define ISSUE(sl, tk) do { const int t_ = (tk); const bool v_ = t_ >= nblk; int i_ = (v_ ? t_ - nblk : t_) + rot; i_ = i_ >= nblk ? i_ - nblk : i_; \
    const bool live_ = t_ < 2 * nblk; i_ = live_ ? i_ : 0; \
    const __amdgpu_buffer_rsrc_t rs_ = rsrc((v_ ? vb : kb) + (size_t)(u32)(i_ * 4096), live_ ? 4096u : 0u); \
    _Pragma("unroll") for (int u_ = 0; u_ < 4; ++u_) buf[(sl) * 4 + u_] = __builtin_amdgcn_raw_buffer_load_b128(rs_, lo + u_ * 1024, 0, 2); \
    id[sl] = t_; } while (0)
    WAIT_Q();
    const u32x4 q = *(const u32x4*)&lds[L_Q + 4 * c];
    if (wave == 0) TS(1);
    float acc[8];
#pragma unroll
    for (int k = 0; k < 8; ++k) acc[k] = 0.f;
    float mx = -INFINITY, Ml = 0.f;
    bool inK = true;
    int last = 0;
    const u32 tk_addr = (u32)(uintptr_t)(LAS u32*)&lds[lane == 0 ? C_TK : L_PART + wave * 128 + lane], tk_inc = lane == 0 ? 1u : 0u;
    const u32 pc_addr = (u32)(uintptr_t)(LAS u32*)&lds[C_PACE];
#define PROG(cur) do {} while (0)
#define STEP(sl) { \
      const int cur = id[sl]; \
      if (cur >= nblk && inK) {     \
        inK = false; \
        KDONE(mx); \
        _Pragma("unroll 1") for (u32 spin = 0; spin < SPIN_CAP * 16; ++spin) if (*(volatile LAS u32*)&lds[C_KDONE] == NKW) break; \
        asm volatile("" ::: "memory"); \
        Ml = asf(lds[C_MX]) * LOG2E; \
        if (wave == 0) TS(2); \
      } \
      if (cur < nblk) { \
        KSCORE(&buf[(sl) * 4], cur, q, mx); \
      } else if (cur < 2 * nblk) { \
        const f32x4 s4 = *(const f32x4*)&lds[L_SC + (cur - nblk) * 16 + 4 * r]; \
        _Pragma("unroll") for (int t = 0; t < 4; ++t) { \
          const float p = __builtin_amdgcn_exp2f(fmaf(s4[t], LOG2E, -Ml)); \
          const u32x4 v = buf[(sl) * 4 + t]; \
          _Pragma("unroll") for (int e = 0; e < 4; ++e) { \
            acc[2 * e] = fmaf(p, asf(v[e] << 16), acc[2 * e]); acc[2 * e + 1] = fmaf(p, asf(v[e] & 0xffff0000u), acc[2 * e + 1]); } } \
      } \
      PROG(cur); \
      if (last < 2 * nblk) { \
        u32 tk; u32x2 pc; u64 now; \
        asm volatile("ds_add_rtn_u32 %0, %3, %4\n\tds_read_b64 %1, %5\n\ts_memrealtime %2\n\ts_waitcnt lgkmcnt(0)" \
                     : "=&v"(tk), "=&v"(pc), "=s"(now) : "v"(tk_addr), "v"(tk_inc), "v"(pc_addr) : "memory");     \
        last = __builtin_amdgcn_readfirstlane(tk); \
        if (PACE_ON && last < 2 * nblk) {     \
          const int tgo = (int)__builtin_amdgcn_readfirstlane(pc[1]) + (int)(((u32)last * __builtin_amdgcn_readfirstlane(pc[0])) >> 8); \
          if ((int)((u32)now - tstart) < tgo) \
            while ((int)((u32)__builtin_amdgcn_s_memrealtime() - tstart) < tgo) __builtin_amdgcn_s_sleep(1); \
        } \
      } \
      ISSUE(sl, last); \
      __builtin_amdgcn_sched_barrier(0); \
    }
#pragma unroll 1
    do { STEP(0) STEP(1) STEP(2) STEP(3) } while (id[0] < 2 * nblk || inK);
    const float b0_ = sw32add(acc[0], acc[4]), b1_ = sw32add(acc[1], acc[5]), b2_ = sw32add(acc[2], acc[6]), b3_ = sw32add(acc[3], acc[7]);
    const f32x2 e = {sw16add(b0_, b2_), sw16add(b1_, b3_)};
    *(f32x2*)&lds[L_PART + wave * 128 + 8 * c + 2 * r] = e;
    TS(4 + wave);
    bar();
    return;
  }

  if (wave < 13) {
    const int j = wave - 4;
    __builtin_amdgcn_s_setprio(2);
    const u32 qrow = 144 * (sp >> 1) + 16 * j + (lane >> 3);
    u32x4 wqv[2];
    {
      const __amdgpu_buffer_rsrc_t ra = rsrc((u64)wq + (head * 256 + (sp & 1) * 128), D * NH * DH * 2 - 8064);
      const u32 oa = qrow * 8192 + (lane & 7) * 16;
      wqv[0] = __builtin_amdgcn_raw_buffer_load_b128(ra, oa, 0, AUX_W);
      wqv[1] = __builtin_amdgcn_raw_buffer_load_b128(ra, oa + 8 * 8192, 0, AUX_W);
    }
    ORDER();
    ARGS_READY();
    const u32 tstart = (u32)__builtin_amdgcn_s_memrealtime();
    SPLIT();
    {
#pragma unroll 1
      for (u32 spin = 0; spin < SPIN_CAP * 16; ++spin)
        if (*(volatile LAS u32*)&lds[C_XRDY] != 0u) break;
      asm volatile("" ::: "memory");
      const u32 xp = (u32)((const u16*)&lds[L_XN])[qrow] | ((u32)((const u16*)&lds[L_XN])[qrow + 8] << 16);
      float qa[8];
#pragma unroll
      for (int e = 0; e < 4; ++e) { qa[2 * e] = dot2(lo2(wqv[0][e], wqv[1][e]), xp, 0.f); qa[2 * e + 1] = dot2(hi2(wqv[0][e], wqv[1][e]), xp, 0.f); }
      lds[L_QPART + j * 64 + 8 * (lane & 7) + 2 * r + (c >> 3)] = asu(reduce8(qa, c));
    }
    asm volatile("s_waitcnt lgkmcnt(0)" ::: "memory");
    if (lane == 0) __hip_atomic_fetch_add((LAS u32*)&lds[C_QCNT], 1u, __ATOMIC_RELAXED, __HIP_MEMORY_SCOPE_WORKGROUP);
    ORDER();
    {
      u32x4 kpre[4 * PRE];
#pragma unroll
      for (int u = 0; u < PRE; ++u) {
        int i = 16 + PRE * j + u + rot; i = i >= nblk ? i - nblk : i;
        const __amdgpu_buffer_rsrc_t rs = rsrc((u64)Kc + kvoff + (u32)(i * 4096), 4096u);
#pragma unroll
        for (int t = 0; t < 4; ++t) kpre[4 * u + t] = __builtin_amdgcn_raw_buffer_load_b128(rs, lo + t * 1024, 0, 2);
      }
      ORDER();
      WAIT_Q();
      const u32x4 q = *(const u32x4*)&lds[L_Q + 4 * c];
      float mx = -INFINITY;
#pragma unroll
      for (int u = 0; u < PRE; ++u) KSCORE(&kpre[4 * u], 16 + PRE * j + u, q, mx);
      asm volatile("s_waitcnt lgkmcnt(0)" ::: "memory");
      KDONE(mx);
    }
    ORDER();
    const int u9 = sp == 7 ? hh : sp == 6 ? 4 + hh : 8, mslot = 9 * xg + u9, nn = 8 * xg + u9 - 2;
    const int cg = u9 == 0 ? 8 * xg + 3 : nn < 0 ? 67 : 8 * (nn / 7) + (nn % 7) + ((nn % 7) >= 3 ? 1 : 0);
    u32x4 W[4][4];
    u32x4* wdl = (u32x4*)&lds[L_WD + j * 2048] + lane;
    {
      const __amdgpu_buffer_rsrc_t ro = rsrc(wo, NH * DH * D * 2), rd = rsrc(wd, mlp ? DF * D * 2 : 0);
      const u32 ovo = r * (32 * D * 2) + c * 8, oso = head * (128 * D * 2) + j * 128;
      const u32 dvo = r * (16 * D * 2) + c * 8 + (mlp ? cg * (64 * D * 2) + j * 128 : 0);
      u32x4 T[4];
#pragma unroll
      for (int k = 0; k < 8; ++k) {
        const u32 tgo = PH_T0 + PH_DT * (9 * k + j);
#pragma unroll 1
        while ((u32)__builtin_amdgcn_s_memrealtime() - tstart < tgo) __builtin_amdgcn_s_sleep(4);
        ORDER();
        u32x2 wov[4], wdv[2];
#pragma unroll
        for (int i = 0; i < 4; ++i) wov[i] = __builtin_amdgcn_raw_buffer_load_b64(ro, ovo, oso + (4 * k + i) * (D * 2), AUX_W);
#pragma unroll
        for (int i = 0; i < 2; ++i) wdv[i] = __builtin_amdgcn_raw_buffer_load_b64(rd, dvo + (2 * k + i) * (D * 2), 0, AUX_W);
        ORDER();
        put_pair(W[k >> 1], 2 * k, wov[0], wov[1]); put_pair(W[k >> 1], 2 * k + 1, wov[2], wov[3]);
        put_pair(T, k, wdv[0], wdv[1]);
        if (k & 1) PIN4(W[k >> 1]);
        if ((k & 3) == 3) {
#pragma unroll
          for (int e = 0; e < 4; ++e) wdl[((k >> 2) * 4 + e) * 64] = T[e];
        }
      }
    }
    float xv = asf((u32)x[64 * j + lane] << 16);
    asm volatile("" : "+v"(xv));
    if (j == 8) TS(3);
    bar();
    if (j == 0) TS(8);
    {
      const float f = asf(lds[C_F]);
      const f32x2 a = (*(const f32x2*)&lds[L_PART + 2 * lane] + *(const f32x2*)&lds[L_PART + 128 + 2 * lane]) +
                      (*(const f32x2*)&lds[L_PART + 256 + 2 * lane] + *(const f32x2*)&lds[L_PART + 384 + 2 * lane]);
      const u32 ap = pk2(f * a[0], f * a[1]);
      f32x4 d[4] = {};
      mstep(afrag<0, 1>(ap), W[0], d); mstep(afrag<1, 1>(ap), W[1], d); mstep(afrag<2, 1>(ap), W[2], d); mstep(afrag<3, 1>(ap), W[3], d);
      __builtin_amdgcn_raw_buffer_store_b128(mout(d), rsrc(ws + OFF_Y1 + chunk * ROWB + 256 * j, 256), lo, 0, 16);
    }
    if (j == 0) TS(9);
    if (!mlp) return;
    bar();
    u32x4 G[2][4], U[2][4];
    {
      const __amdgpu_buffer_rsrc_t rg = rsrc(wg, D * DF * 2), ru = rsrc(wu, D * DF * 2);
      const u32 vo = r * (16 * DF * 2) + c * 8, so = (64 * j) * (DF * 2) + cg * 128;
      u32x2 gv[16], uv[16];
#pragma unroll
      for (int i = 0; i < 16; ++i) gv[i] = __builtin_amdgcn_raw_buffer_load_b64(rg, vo, so + i * (DF * 2), AUX_W);
#pragma unroll
      for (int i = 0; i < 16; ++i) uv[i] = __builtin_amdgcn_raw_buffer_load_b64(ru, vo, so + i * (DF * 2), AUX_W);
      ORDER();
#pragma unroll
      for (int p = 0; p < 8; ++p) put_pair(G[p >> 2], p, gv[2 * p], gv[2 * p + 1]);
      PIN4(G[0]); PIN4(G[1]);
#pragma unroll
      for (int p = 0; p < 8; ++p) put_pair(U[p >> 2], p, uv[2 * p], uv[2 * p + 1]);
      PIN4(U[0]); PIN4(U[1]);
    }
    u32x4 WD[2][4];
#pragma unroll
    for (int k = 0; k < 8; ++k) WD[k >> 2][k & 3] = wdl[k * 64];
    PIN4(WD[0]); PIN4(WD[1]);
    if (j == 8) TS(17);
    float hv;
    {
      const __amdgpu_buffer_rsrc_t rp = rsrc(ws + OFF_P + xg * PREP_BYTES, PREP_BYTES);
      u32 v[8];
#pragma unroll 1
      for (u32 spin = 0; spin < SPIN_CAP; ++spin) {
#pragma unroll
        for (int k = 0; k < 8; ++k) v[k] = __builtin_amdgcn_raw_buffer_load_b32(rp, lane * 4, k * ROWB + 256 * j, 16);
        __builtin_amdgcn_sched_barrier(0);
        const u32 mxu = max(max(max(v[0], v[1]), max(v[2], v[3])), max(max(v[4], v[5]), max(v[6], v[7])));
        if (!__any(mxu == SENT)) break;
        asm volatile("s_sleep 1" ::: "memory");
      }
      hv = rbf(xv + rbf(((asf(v[0]) + asf(v[1])) + (asf(v[2]) + asf(v[3]))) + ((asf(v[4]) + asf(v[5])) + (asf(v[6]) + asf(v[7])))));
    }
    if (j == 0) TS(11);
    {
      const float pv = asf(__builtin_amdgcn_readfirstlane(asu(hv))), dj = hv - pv;
      const float s1 = wave_sum(dj), s2 = wave_sum(dj * dj);
      if (lane == 0) *(f32x2*)&lds[L_ST + 2 * j] = f32x2{fmaf(s1, 1.0f / 64, pv), fmaf(-s1 * (1.0f / 64), s1, s2)};
    }
    bar();
    if (j == 0) TS(12);
    {
      const f32x2 st = *(const f32x2*)&lds[L_ST + 2 * (c < 9 ? c : 0)];
      const float m0 = asf(lds[L_ST]), dm = st[0] - m0;
      const float t1 = row_sum(dm), t2 = row_sum(c < 9 ? fmaf(64.f * dm, dm, st[1]) : 0.f);
      const float mean = fmaf(t1, 1.0f / 9, m0), var = fmaxf(fmaf(-t1 * (64.0f / 9), t1, t2), 0.f) * (1.0f / D);
      float mb, rden;
      ln_finish(mean, var, mb, rden);
      const u32 hp = pair_up(rbf(hv - mb) * rden);
      const bf16x8 a0 = afrag<0, 2>(hp), a1 = afrag<1, 2>(hp);
      f32x4 dg[4] = {}, du[4] = {};
      mstep(a0, G[0], dg); mstep(a1, G[1], dg); mstep(a0, U[0], du); mstep(a1, U[1], du);
      f32x4 o;
#pragma unroll
      for (int e = 0; e < 4; ++e) o[e] = (r & 1) ? du[e][0] : dg[e][0];
      if (lane < 32) *(f32x4*)&lds[L_GU + j * 128 + 4 * lane] = o;
    }
    bar();
    {
      f32x2 p[9];
#pragma unroll
      for (int k = 0; k < 9; ++k) p[k] = f32x2{asf(lds[L_GU + k * 128 + lane]), asf(lds[L_GU + k * 128 + 64 + lane])};
      const f32x2 s = (((p[0] + p[1]) + (p[2] + p[3])) + ((p[4] + p[5]) + (p[6] + p[7]))) + p[8];
      const float gg = rbf(s[0]), uu = rbf(s[1]);
      const float t = gg * fmaf(gg * gg, 0.044715f, 1.0f) * (-2.0f * 0.7978845608028654f * LOG2E);
      const float ge = rbf(gg * __builtin_amdgcn_rcpf(1.0f + __builtin_amdgcn_exp2f(t)));
      const u32 ap = pair_up(ge * uu);
      f32x4 d[4] = {};
      mstep(afrag<0, 2>(ap), WD[0], d); mstep(afrag<1, 2>(ap), WD[1], d);
      __builtin_amdgcn_raw_buffer_store_b128(mout(d), rsrc(ws + OFF_Y3 + mslot * ROWB + 256 * j, 256), lo, 0, 16);
    }
    if (j == 0) TS(13);
    return;
  }

  __builtin_amdgcn_s_setprio(3);
  if (wave == 14) {
    u32x2 e2;
    u32 tstart;
    {
      const __amdgpu_buffer_rsrc_t rx = rsrc(x, D * 2);
      const u32x4 xa = __builtin_amdgcn_raw_buffer_load_b128(rx, lo, 0, 0), xb = __builtin_amdgcn_raw_buffer_load_b128(rx, lo + 1024, 0, 0);
      u32 z = 0;
      asm volatile("" : "+v"(z));
      e2 = __builtin_amdgcn_raw_buffer_load_b64(rsrc(ws + OFF_EPOCH, 8), z, 0, 16);
      ORDER();
      ARGS_READY();
      tstart = (u32)__builtin_amdgcn_s_memrealtime();
      float xv[16];
#pragma unroll
      for (int e = 0; e < 4; ++e) {
        xv[2 * e] = asf(xa[e] << 16); xv[2 * e + 1] = asf(xa[e] & 0xffff0000u);
        xv[8 + 2 * e] = asf(xb[e] << 16); xv[9 + 2 * e] = asf(xb[e] & 0xffff0000u);
      }
      float s0 = 0.f, s1 = 0.f;
#pragma unroll
      for (int i = 0; i < 8; ++i) { s0 += xv[i]; s1 += xv[8 + i]; }
      const float mean = wave_sum(s0 + s1) * (1.0f / D);
      float sq = 0.f, sq2 = 0.f;
#pragma unroll
      for (int i = 0; i < 8; ++i) { const float d = xv[i] - mean, d2 = xv[8 + i] - mean; sq = fmaf(d, d, sq); sq2 = fmaf(d2, d2, sq2); }
      if (lane < 8) sq += sq2;
      float mb, rden;
      ln_finish(mean, wave_sum(sq) * (1.0f / D), mb, rden);
      u32x4 oa, ob;
#pragma unroll
      for (int e = 0; e < 4; ++e) {
        oa[e] = pk2(rbf(xv[2 * e] - mb) * rden, rbf(xv[2 * e + 1] - mb) * rden);
        ob[e] = pk2(rbf(xv[8 + 2 * e] - mb) * rden, rbf(xv[9 + 2 * e] - mb) * rden);
      }
      *(u32x4*)&lds[L_XN + 4 * lane] = oa;
      if (lane < 8) *(u32x4*)&lds[L_XN + 256 + 4 * lane] = ob;
      asm volatile("s_waitcnt lgkmcnt(0)" ::: "memory");
      if (lane == 0) *(volatile LAS u32*)&lds[C_XRDY] = 1u;
    }
    const u64 tag = next_tag(((u64)e2[1] << 32) | e2[0]);
    const u32 tl = (u32)tag & 0xffff0000u, th = (u32)(tag >> 32);
    const __amdgpu_buffer_rsrc_t rq = rsrc(ws + OFF_Q + head * 8192, 8192);
#pragma unroll 1
    for (u32 spin = 0; spin < SPIN_CAP * 16; ++spin)
      if (*(volatile LAS u32*)&lds[C_QCNT] == 9u) break;
    asm volatile("" ::: "memory");
    float a = 0.f;
#pragma unroll
    for (int j = 0; j < 9; ++j) a += asf(lds[L_QPART + j * 64 + lane]);
    __builtin_amdgcn_raw_buffer_store_b128(u32x4{tl | (asu(a) & 0xffffu), th, tl | (asu(a) >> 16), th}, rq, sp * 1024 + lo, 0, 16);
    TS(16);
    __builtin_amdgcn_s_sleep(QDELAY);
    u32 off[8];
    float v[8];
#pragma unroll
    for (int k = 0; k < 8; ++k) { off[k] = k * 1024 + lo; v[k] = 0.f; }
#pragma unroll 1
    for (u32 spin = 0; spin < SPIN_CAP; ++spin) {
      u32x4 n[8];
#pragma unroll
      for (int k = 0; k < 8; ++k) n[k] = __builtin_amdgcn_raw_buffer_load_b128(rq, off[k], 0, 16);
      __builtin_amdgcn_sched_barrier(0);
      bool pend = false;
#pragma unroll
      for (int k = 0; k < 8; ++k) {
        const bool live = off[k] != OOB;
        const bool ok = (((n[k][0] ^ tl) | (n[k][2] ^ tl)) & 0xffff0000u) == 0u && n[k][1] == th && n[k][3] == th;
        if (live) v[k] = asf(lo2(n[k][0], n[k][2]));
        if (ok) off[k] = OOB;
        pend |= live && !ok;
      }
      if (!__any(pend)) break;
      asm volatile("s_sleep 1" ::: "memory");
    }
    ((u16*)&lds[L_Q])[lane] = (u16)pk2((v[0] + v[2]) + (v[4] + v[6]), 0.f);
    ((u16*)&lds[L_Q])[64 + lane] = (u16)pk2((v[1] + v[3]) + (v[5] + v[7]), 0.f);
    asm volatile("s_waitcnt lgkmcnt(0)" ::: "memory");
    if (lane == 0) *(volatile LAS u32*)&lds[C_QRDY] = 1u;
    {
      SPLIT();
      const __amdgpu_buffer_rsrc_t rr = rsrc(ws + OFF_PR, 8 * 4096);
      const float span = (float)(2 * nblk - (int)TK0);
      float f0 = 0.f;
      int k0 = -1;
#pragma unroll 1
      for (int k = 0; k < PC_N; ++k) {
        const u32 tk = PC_T0 + k * PC_DT;
#pragma unroll 1
        while ((u32)__builtin_amdgcn_s_memrealtime() - tstart < tk) __builtin_amdgcn_s_sleep(2);
        if (sp == 0) {
          const u32 fr = min((u32)((float)(*(volatile LAS u32*)&lds[C_TK] - TK0) * (65535.f / span)), 65535u);
          __builtin_amdgcn_raw_buffer_store_b64(u32x2{tl | fr, th}, rr, lane == 0 ? k * 4096 + head * 128 : OOB, 0, 16);
        }
#pragma unroll 1
        while ((u32)__builtin_amdgcn_s_memrealtime() - tstart < tk + PC_LAG) __builtin_amdgcn_s_sleep(8);
        asm volatile("" ::: "memory");
        const u32x2 g = __builtin_amdgcn_raw_buffer_load_b64(rr, lane < 32 ? k * 4096 + lane * 128 : OOB, 0, 16);
        const bool ok = lane < 32 && (g[0] & 0xffff0000u) == tl && g[1] == th;
        const float cnt = wave_sum(ok ? 1.f : 0.f), sum = wave_sum(ok ? (float)(g[0] & 0xffffu) : 0.f);
        if (cnt < 24.f) continue;
        const float f = sum / (cnt * 65535.f);
        if (k0 < 0) { f0 = f; k0 = k; continue; }
        const float rate = (f - f0) * (1.f + PC_EPS) / (float)((k - k0) * PC_DT);
        if (rate < 1e-4f) continue;
        const float a = 1.f / (rate * span * (mlp ? 1.f + PC_LEAD : 1.f));
        const float bq = (float)tk - (f + PC_MARG) / rate - a * (float)TK0;
        if (lane == 0) *(volatile LAS u64*)&lds[C_PACE] = ((u64)(u32)(int)bq << 32) | (u32)(a * 256.f);
      }
    }
    return;
  }

  if (wave == 15 && lane < 9) lds[C_MX + lane] = lane == 0 ? 0xFF800000u : lane == 1 ? TK0 : 0u;
  {
    const u32x4 cpf = __builtin_amdgcn_raw_buffer_load_b128(rsrc(pc0 + (2 * idx + (wave >> 1 & 1)) * CODE_SLICE, CODE_SLICE), lo, 0, 0);
    ORDER();
    ARGS_READY();
    asm volatile("" :: "v"(cpf));
  }
#define RED() \
  int hh_ = hh; asm volatile("" : "+s"(hh_)); \
  const int rslot = 5 * hh_ + sp; \
  const bool red = sp < 5 && rslot < 18; \
  const u32 poff = (lane >> 3) * PREP_BYTES + xg * ROWB + rslot * 128 + (lane & 7) * 16
  if (wave == 13) {
    RED();
    if (!red) return;
    const int col0 = 72 * xg + 4 * rslot;
    __builtin_amdgcn_s_sleep(127);
    float xv = asf((u32)x[col0 + r] << 16);
    u64 tag = 0;
    if (b0 == 0) tag = next_tag(__hip_atomic_load((GAS u64*)(ws + OFF_EPOCH), RLX_AGENT));
    asm volatile("" : "+v"(xv));
    bar();
    __builtin_amdgcn_s_sleep(DELAY1);
    const __amdgpu_buffer_rsrc_t rp = rsrc(ws + OFF_P, 8 * PREP_BYTES);
    {
      u32 off[4];
      u32x4 v[4];
#pragma unroll
      for (int k = 0; k < 4; ++k) off[k] = (u32)(8 * k + (lane >> 3)) * ROWB + rslot * 128 + (lane & 7) * 16;
      gather<4>(rsrc(ws + OFF_Y1 + xg * 32 * ROWB, 32 * ROWB), off, v);
      u32x4 o;
#pragma unroll
      for (int e = 0; e < 4; ++e) {
        float s = (asf(v[0][e]) + asf(v[1][e])) + (asf(v[2][e]) + asf(v[3][e]));
        s += dpp<ROR8>(s); s = sw16add(s, s); s = sw32add(s, s);
        o[e] = canon(s);
      }
      __builtin_amdgcn_raw_buffer_store_b128(o, rp, poff, 0, 16);
    }
    TS(10);
    float hv;
    {
      u32 off[1] = {lane < 8 ? xg * PREP_BYTES + lane * ROWB + col0 * 4 : OOB};
      u32x4 v[1];
      gather<1>(rp, off, v);
      hv = rbf(xv + rbf(reduce4(v[0], u32x4{0u, 0u, 0u, 0u})));
    }
    __builtin_amdgcn_s_sleep(DELAY3);
    {
      u32 off[2] = {lane * ROWB + col0 * 4, lane < 8 ? (64 + lane) * ROWB + col0 * 4 : OOB};
      u32x4 v[2];
      gather<2>(rsrc(ws + OFF_Y3, NMLP * ROWB), off, v);
      const float o = hv + rbf(reduce4(v[0], v[1]));
      if (c == 0) out[col0 + r] = (u16)pk2(o, 0.f);
    }
    if (b0 == 0 && lane == 0) *(GAS u64*)(ws + OFF_EPOCH) = tag;
    TS(14);
    return;
  }

  {
    __builtin_amdgcn_s_sleep(127);
    SPLIT();
    RED();
    const u64 tag = next_tag(__hip_atomic_load((GAS u64*)(ws + OFF_EPOCH), RLX_AGENT));
    {
      const int mslot = 9 * xg + (sp == 7 ? hh : sp == 6 ? 4 + hh : 8);
      const __amdgpu_buffer_rsrc_t r1 = rsrc(ws + OFF_Y1 + chunk * ROWB, ROWB), r3 = rsrc(ws + OFF_Y3 + mslot * ROWB, mlp ? ROWB : 0);
      const u32x4 s4 = {SENT, SENT, SENT, SENT};
#pragma unroll
      for (int k = 0; k < 3; ++k) {
        __builtin_amdgcn_raw_buffer_store_b128(s4, r1, k * 1024 + lo, 0, 16);
        __builtin_amdgcn_raw_buffer_store_b128(s4, r3, k * 1024 + lo, 0, 16);
      }
      __builtin_amdgcn_raw_buffer_store_b128(s4, rsrc(ws + OFF_P, red ? 8 * PREP_BYTES : 0), poff, 0, 16);
      if (lane < 2) st_sc1(ws + OFF_ML + chunk * 32 + lane * 4, SENT);
    }
    asm volatile("s_waitcnt vmcnt(0)" ::: "memory");
    if (lane == 0) __hip_atomic_store((GAS u64*)(ws + OFF_FLAGS + b0 * 8), tag, RLX_AGENT);
#pragma unroll 1
    for (u32 spin = 0; spin < SPIN_CAP; ++spin) {
      asm volatile("s_sleep 64" ::: "memory");
      bool bad = false;
#pragma unroll
      for (int k = 0; k < 4; ++k) bad |= __hip_atomic_load((GAS u64*)(ws + OFF_FLAGS + (lane + 64 * k) * 8), RLX_AGENT) != tag;
      if (!__any(bad)) break;
    }
#pragma unroll 1
    for (u32 spin = 0; spin < SPIN_CAP; ++spin) {
      asm volatile("s_sleep 32" ::: "memory");
      if (*(volatile LAS u32*)&lds[C_KDONE] == NKW) break;
    }
    asm volatile("" ::: "memory");
    {
      const float mw = asf(lds[C_MX]), Ml = mw * LOG2E;
      float l = 0.f;
#pragma unroll 1
      for (int i = lane; i < nblk * 16; i += 64) l += __builtin_amdgcn_exp2f(fmaf(asf(lds[L_SC + i]), LOG2E, -Ml));
      l = wave_sum(l);
      if (lane == 0) __hip_atomic_store((GAS u64*)(ws + OFF_ML + chunk * 32), ((u64)canon(l) << 32) | canon(mw), RLX_AGENT);
    }
    float m, l;
#pragma unroll 1
    for (u32 spin = 0; spin < SPIN_CAP; ++spin) {
      asm volatile("s_sleep 64" ::: "memory");
      const u64 v = __hip_atomic_load((GAS u64*)(ws + OFF_ML + head * 256 + (lane & 7) * 32), RLX_AGENT);
      m = asf((u32)v); l = asf((u32)(v >> 32));
      if (!__any((u32)v == SENT || (u32)(v >> 32) == SENT)) break;
    }
    const float M = wave_max(m);
    const float e = __builtin_amdgcn_exp2f((m - M) * LOG2E);
    const float L = row_sum(e * l) * 0.5f;
    if (lane == sp) lds[C_F] = asu(e / L);
    TS(15);
    bar();
  }
}

extern "C" void kernel_launch(void* const* d_in, const int* in_sizes, int n_in, void* d_out, int out_size, void* d_ws, size_t ws_size,
                              hipStream_t stream) {
  fused_decode<<<NWG, NTHR, 0, stream>>>((const u16*)d_in[0], (const u16*)d_in[1], (const u16*)d_in[3], (unsigned char*)d_ws,
                                         (const u16*)d_in[2], (const u16*)d_in[4], (const u16*)d_in[6], (const u16*)d_in[7],
                                         (const u16*)d_in[8], (u16*)d_out);
}
```
